```python
import torch

def _softmax(x, axis=-1):
    return torch.softmax(x, dim=axis)
def _gelu(x):
    return torch.nn.functional.gelu(x, approximate="tanh")
def _silu(x):
    return torch.nn.functional.silu(x)
def _sigmoid(x):
    return torch.sigmoid(x)
def _layernorm(x, axis=-1, eps=1e-5):
    m = x.mean(dim=axis, keepdim=True)
    v = x.var(dim=axis, keepdim=True, unbiased=False)
    return (x - m) / torch.sqrt(v + eps)
def _scatter_add(tgt, idx, src, axis=0):
    return tgt.clone().index_add_(axis, idx, src)
def _segment_sum(src, seg, n):
    out = torch.zeros((n,) + src.shape[1:], dtype=src.dtype)
    return out.index_add_(0, seg, src)
def _pad(x, pw):
    flat = tuple(v for pair in reversed(pw) for v in pair)
    return torch.nn.functional.pad(x, flat)

kernel_name = 'standalone__gqa_softmax__near_ridge'

def setup_inputs():

    _w = (lambda a: (lambda t: t.to(torch.bfloat16) if t.is_floating_point() else t)(torch.as_tensor(a)))
    B, Hq, Hkv, Sq, Sk, D = 8, 32, 8, 320, 320, 128
    return {
        "q": _w((lambda *s: torch.randn(*s, dtype=torch.bfloat16) * 0.8)(B, Hq, Sq, D)),
        "k": _w((lambda *s: torch.randn(*s, dtype=torch.bfloat16) * 0.8)(B, Hkv, Sk, D)),
        "v": _w((lambda *s: torch.randn(*s, dtype=torch.bfloat16) * 0.05)(B, Hkv, Sk, D)),
    }

def reference(q, k, v):
    B, Hq, Sq, D = q.shape
    Hkv = k.shape[1]
    rep = Hq // Hkv
    k = (lambda x, n, axis: torch.repeat_interleave(x, n, dim=axis))(k, rep, axis=1)
    v = (lambda x, n, axis: torch.repeat_interleave(x, n, dim=axis))(v, rep, axis=1)
    scale = D ** -0.5
    scores = torch.einsum("bhqd,bhkd->bhqk", q, k) * scale
    p = _softmax(scores, axis=-1)
    return torch.einsum("bhqk,bhkd->bhqd", p, v)
```

```cpp
#include <hip/hip_runtime.h>
#include <hip/hip_bf16.h>
#include <utility>

typedef __attribute__((ext_vector_type(8))) __bf16 bf16x8;
typedef __attribute__((ext_vector_type(4))) __bf16 bf16x4;
typedef __attribute__((ext_vector_type(2))) __bf16 bf16x2;
typedef __attribute__((ext_vector_type(16))) float f32x16;
typedef __attribute__((ext_vector_type(4))) float f32x4;
typedef __attribute__((ext_vector_type(2))) float f32x2;
typedef __attribute__((ext_vector_type(4))) unsigned u32x4;
typedef __attribute__((ext_vector_type(2))) unsigned u32x2;
#define LDS3 __attribute__((address_space(3)))
#define DI __device__ __forceinline__
#define GAP() __builtin_amdgcn_sched_barrier(0)
template <int... Is> using iseq = std::integer_sequence<int, Is...>;
template <int N> using make_iseq = std::make_integer_sequence<int, N>;

constexpr int SEQ = 320, DH = 128;
constexpr unsigned HEAD_BYTES = SEQ * DH * 2;
constexpr unsigned HALF_BYTES = HEAD_BYTES / 2;
constexpr unsigned VOFF = HEAD_BYTES;
constexpr float CEXP = 0.088388347648318440f * 1.4426950408889634f;
constexpr int PD = 3;

#define ST_AUX 0

DI unsigned cvtpk(float lo, float hi) {
  f32x2 v = {lo, hi}; bf16x2 b = __builtin_convertvector(v, bf16x2); return __builtin_bit_cast(unsigned, b);
}
DI float max3f(float a, float b, float c) { return __builtin_fmaxf(__builtin_fmaxf(a, b), c); }
DI float xhalf_max(float x) {
  auto r = __builtin_amdgcn_permlane32_swap(__float_as_uint(x), __float_as_uint(x), false, false);
  return __builtin_fmaxf(__uint_as_float(r[0]), __uint_as_float(r[1]));
}
DI float xhalf_sum(float x) {
  auto r = __builtin_amdgcn_permlane32_swap(__float_as_uint(x), __float_as_uint(x), false, false);
  return __uint_as_float(r[0]) + __uint_as_float(r[1]);
}
DI bf16x8 ldK(unsigned base, int off) { return *(const LDS3 bf16x8*)(base + off); }
DI bf16x8 ldV(unsigned base, int off) {
  bf16x4 a = __builtin_amdgcn_ds_read_tr16_b64_v4bf16((LDS3 bf16x4*)(base + off));
  bf16x4 b = __builtin_amdgcn_ds_read_tr16_b64_v4bf16((LDS3 bf16x4*)(base + off + 2048));
  return __builtin_shufflevector(a, b, 0, 1, 2, 3, 4, 5, 6, 7);
}

struct Ctx {
  unsigned kb[2][2];
  unsigned vb[2];
  bf16x8 q[8];
  bf16x8 fr[PD + 1];
  f32x16 S[2][5];
  u32x4 P[2][10];
  f32x16 O[4];
  float mx, mc[2], m0, alpha, l;
  unsigned isA;
};

template <int G> DI bf16x8 frag(const Ctx& c) {
  if constexpr (G < 80) { constexpr int hf = G / 40, i = G % 40, t = i >> 3, s = i & 7; return ldK(c.kb[hf][s & 1], t * 8192 + (s >> 1) * 256); }
  else { constexpr int hf = (G - 80) / 40, i = (G - 80) % 40, ks = i >> 2, db = i & 3; return ldV(c.vb[hf], ks * 4096 + db * 256); }
}
template <int HF, int E0, int E1> DI void sm_range(Ctx& c) {
#pragma unroll
  for (int e = E0; e < E1; e += 2) {
    const float a = __builtin_amdgcn_exp2f(__builtin_fmaf(c.S[HF][e >> 4][e & 15], CEXP, c.mc[HF]));
    const float b = __builtin_amdgcn_exp2f(__builtin_fmaf(c.S[HF][e >> 4][(e & 15) + 1], CEXP, c.mc[HF]));
    c.l += a; c.l += b;
    if ((e & 7) == 0) asm volatile("" : "=v"(c.P[HF][e >> 3]));
    c.P[HF][e >> 3][(e >> 1) & 3] = cvtpk(a, b);
  }
}
template <int HF, int T, int I0, int I1> DI void max_range(Ctx& c) {
#pragma unroll
  for (int i = I0; i < I1; ++i) c.mx = max3f(c.mx, c.S[HF][T][2 * i], c.S[HF][T][2 * i + 1]);
}
#define CONDBAR(flag, N) asm volatile("s_cmp_gt_u32 %0, 3\n\ts_cbranch_scc1 1f\n\ts_waitcnt vmcnt(" #N ")\n\ts_barrier\n1:" :: "s"(flag) : "memory", "scc")

template <int G> DI void step(Ctx& c) {
  if constexpr (G + PD == 40) CONDBAR(c.isA, 10);
  if constexpr (G + PD == 80) CONDBAR(c.isA, 5);
  if constexpr (G + PD == 120) CONDBAR(c.isA, 0);
  if constexpr (G + PD < 160) c.fr[(G + PD) % (PD + 1)] = frag<G + PD>(c);
  const bf16x8 f = c.fr[G % (PD + 1)];
  if constexpr (G < 80) {
    constexpr int hf = G / 40, i = G % 40, t = i >> 3, s = i & 7;
    if constexpr (s == 0) c.S[hf][t] = __builtin_amdgcn_mfma_f32_32x32x16_bf16(f, c.q[s], f32x16{}, 0, 0, 0);
    else c.S[hf][t] = __builtin_amdgcn_mfma_f32_32x32x16_bf16(f, c.q[s], c.S[hf][t], 0, 0, 0);
    if constexpr (t > 0 && s >= 2 && s < 6) max_range<hf, t - 1, 2 * (s - 2), 2 * (s - 2) + 2>(c);
  } else {
    constexpr int hf = (G - 80) / 40, i = (G - 80) % 40, ks = i >> 2, db = i & 3;
    if constexpr (hf == 1 && ks == 0) {
#pragma unroll
      for (int e = 0; e < 16; ++e) c.O[db][e] *= c.alpha;
    }
    if constexpr (G < 84) c.O[db] = __builtin_amdgcn_mfma_f32_32x32x16_bf16(f, __builtin_bit_cast(bf16x8, c.P[hf][ks]), f32x16{}, 0, 0, 0);
    else c.O[db] = __builtin_amdgcn_mfma_f32_32x32x16_bf16(f, __builtin_bit_cast(bf16x8, c.P[hf][ks]), c.O[db], 0, 0, 0);
  }
  if constexpr (G >= 40 && G < 120) {
    constexpr int hf = G / 40 - 1, i = G % 40;
    if constexpr (i == 1) max_range<hf, 4, 0, 8>(c);
    if constexpr (i == 1) {
      const float m = xhalf_max(c.mx);
      c.mx = m; c.mc[hf] = -m * CEXP;
      if constexpr (hf == 0) c.m0 = m;
      else { c.alpha = __builtin_amdgcn_exp2f((c.m0 - m) * CEXP); c.l *= c.alpha; }
    }
    if constexpr (i >= 2) {
      constexpr int e0 = 2 * ((40 * (i - 2)) / 38), e1 = 2 * ((40 * (i - 1)) / 38);
      sm_range<hf, e0, e1>(c);
      if constexpr (((e1 - 1) >> 3) != (e0 >> 3)) asm volatile("" : "+v"(c.l), "+v"(c.P[hf][(e1 - 1) >> 3]), "+v"(c.P[hf][e0 >> 3]));
      else asm volatile("" : "+v"(c.l), "+v"(c.P[hf][e0 >> 3]));
    }
  }
  if constexpr (G < 80) asm volatile("" : "+v"(c.mx));
  GAP();
}
template <int... Gs> DI void steps(Ctx& c, iseq<Gs...>) { (step<Gs>(c), ...); }
template <int B, int... Gs> DI void steps_from(Ctx& c, iseq<Gs...>) { (step<B + Gs>(c), ...); }

template <int G2> DI void store_g(const f32x16& o, float sc, __amdgpu_buffer_rsrc_t srdO, unsigned voff, unsigned soff, int db) {
  constexpr int g = 2 * G2;
  unsigned a0 = cvtpk(o[4 * g + 0] * sc, o[4 * g + 1] * sc), a1 = cvtpk(o[4 * g + 2] * sc, o[4 * g + 3] * sc);
  unsigned b0 = cvtpk(o[4 * g + 4] * sc, o[4 * g + 5] * sc), b1 = cvtpk(o[4 * g + 6] * sc, o[4 * g + 7] * sc);
  auto r0 = __builtin_amdgcn_permlane32_swap(a0, b0, false, false);
  auto r1 = __builtin_amdgcn_permlane32_swap(a1, b1, false, false);
  u32x4 w = {r0[0], r1[0], r0[1], r1[1]};
  __builtin_amdgcn_raw_buffer_store_b128(w, srdO, voff + soff + db * 64 + g * 16, 0, ST_AUX);
}
DI void store_db(const f32x16& o, float sc, __amdgpu_buffer_rsrc_t srdO, unsigned voff, unsigned soff, int db) {
  store_g<0>(o, sc, srdO, voff, soff, db); store_g<1>(o, sc, srdO, voff, soff, db);
}

struct Epi { __amdgpu_buffer_rsrc_t srdO; unsigned voff, soff; float il; };
template <int G> DI bf16x8 ufrag(const Ctx& c) {
  if constexpr (G < 40) { constexpr int t = G >> 3, s = G & 7; return ldK(c.kb[0][s & 1], t * 8192 + (s >> 1) * 256); }
  else { constexpr int i = G - 40, ks = i >> 2, db = i & 3; return ldV(c.vb[0], ks * 4096 + db * 256); }
}
template <int G> DI void ustep(Ctx& c, const Epi& ep) {
  if constexpr (G + PD < 80) c.fr[(G + PD) % (PD + 1)] = ufrag<G + PD>(c);
  const bf16x8 f = c.fr[G % (PD + 1)];
  if constexpr (G < 40) {
    constexpr int t = G >> 3, s = G & 7;
    if constexpr (s == 0) c.S[0][t] = __builtin_amdgcn_mfma_f32_32x32x16_bf16(f, c.q[s], f32x16{}, 0, 0, 0);
    else c.S[0][t] = __builtin_amdgcn_mfma_f32_32x32x16_bf16(f, c.q[s], c.S[0][t], 0, 0, 0);
    if constexpr (t > 0 && s >= 2 && s < 6) max_range<0, t - 1, 2 * (s - 2), 2 * (s - 2) + 2>(c);
    if constexpr ((G & 3) == 1 && G < 32) store_g<(G >> 2) & 1>(c.O[G >> 3], ep.il, ep.srdO, ep.voff, ep.soff, G >> 3);
    asm volatile("" : "+v"(c.mx));
    if constexpr (G == 39) {
      max_range<0, 4, 0, 8>(c);
      const float m = xhalf_max(c.mx);
      c.mx = m; c.mc[0] = -m * CEXP;
      sm_range<0, 0, 8>(c);
    }
  } else {
    constexpr int i = G - 40, ks = i >> 2, db = i & 3;
    if constexpr (ks == 0) c.O[db] = __builtin_amdgcn_mfma_f32_32x32x16_bf16(f, __builtin_bit_cast(bf16x8, c.P[0][ks]), f32x16{}, 0, 0, 0);
    else c.O[db] = __builtin_amdgcn_mfma_f32_32x32x16_bf16(f, __builtin_bit_cast(bf16x8, c.P[0][ks]), c.O[db], 0, 0, 0);
    if constexpr (ks < 9) {
      sm_range<0, 8 + 2 * i, 8 + 2 * i + 2>(c);
      asm volatile("" : "+v"(c.l), "+v"(c.P[0][ks + 1]));
    }
  }
  GAP();
}
template <int... Gs> DI void usteps(Ctx& c, const Epi& ep, iseq<Gs...>) { (ustep<Gs>(c, ep), ...); }

#define QLOAD(qq, soff)                                                                                      \
  do {                                                                                                       \
    _Pragma("unroll") for (int s_ = 0; s_ < 8; ++s_)                                                         \
      asm volatile("buffer_load_dwordx4 %0, %1, %2, %3 offen offset:%4"                                      \
                   : "=v"(qq[s_]) : "v"(qvoff), "s"(srdQ), "s"(soff), "i"(32 * s_) : "memory");              \
  } while (0)
#define QWAIT(qq, N)                                                                                         \
  asm volatile("s_waitcnt vmcnt(" #N ")"                                                                     \
               : "+v"(qq[0]), "+v"(qq[1]), "+v"(qq[2]), "+v"(qq[3]), "+v"(qq[4]), "+v"(qq[5]), "+v"(qq[6]), "+v"(qq[7]) :: "memory")
#define DMA(m0v, voff, srd, soff)                                                                            \
  asm volatile("s_mov_b32 m0, %0\n\ts_nop 0\n\tbuffer_load_dwordx4 %1, %2, %3 offen lds"                     \
               :: "s"(m0v), "v"(voff), "s"(srd), "s"(soff) : "memory")

__global__ __launch_bounds__(512, 2)
void gqa_attn_kernel(const unsigned short* __restrict__ Q, const unsigned short* __restrict__ K,
                     const unsigned short* __restrict__ V, unsigned short* __restrict__ O) {
  __shared__ __attribute__((aligned(16))) char lds[2 * HEAD_BYTES];
  const unsigned ldsb = (unsigned)(size_t)(LDS3 char*)lds;
  const unsigned lane = threadIdx.x & 63u, r32 = lane & 31u, hi = lane >> 5;
  unsigned wid = __builtin_amdgcn_readfirstlane(threadIdx.x >> 6);
  asm volatile("s_nop 4" : "+s"(wid));
  const unsigned bid = blockIdx.x;
  const unsigned grp = (bid & 7u) + 8u * (bid >> 5);
  const unsigned head = grp * 4u + ((bid >> 3) & 3u);
  Ctx c;
  c.isA = wid;

  unsigned pfd = 0;
  if (wid == 7) {
    unsigned long long pc; unsigned len;
    asm volatile("s_getpc_b64 %0\n.Lgqa_pf_start:\n\ts_mov_b32 %1, .Lgqa_pf_end-.Lgqa_pf_start" : "=s"(pc), "=s"(len));
    for (unsigned off = 0; off < len; off += 4096) {
      unsigned o = off + lane * 64u; o = o < len - 4u ? o : len - 4u;
      asm volatile("global_load_dword %0, %1, %2" : "+v"(pfd) : "v"(o), "s"(pc) : "memory");
    }
  }

  __amdgpu_buffer_rsrc_t srdQ = __builtin_amdgcn_make_buffer_rsrc((void*)((const char*)Q + (size_t)head * HEAD_BYTES), (short)0, (int)HEAD_BYTES, 0x00020000);
  __amdgpu_buffer_rsrc_t srdK = __builtin_amdgcn_make_buffer_rsrc((void*)((const char*)K + (size_t)grp * HEAD_BYTES), (short)0, (int)HEAD_BYTES, 0x00020000);
  __amdgpu_buffer_rsrc_t srdV = __builtin_amdgcn_make_buffer_rsrc((void*)((const char*)V + (size_t)grp * HEAD_BYTES), (short)0, (int)HEAD_BYTES, 0x00020000);
  __amdgpu_buffer_rsrc_t srdO = __builtin_amdgcn_make_buffer_rsrc((void*)((char*)O + (size_t)head * HEAD_BYTES), (short)0, (int)HEAD_BYTES, 0x00020000);

  const unsigned qvoff = r32 * 256u + hi * 16u;
  const unsigned ksrc = wid * 1024u + (lane & 3u) * 256u + (((lane >> 2) ^ (wid & 3u)) * 16u);
  const unsigned vsrc = wid * 1024u + ((lane >> 2) & 3u) * 256u + (((lane >> 4) * 4u + (lane & 3u)) * 16u);
  const unsigned qsoff = wid * 8192u;

#define QLOAD_IF(flagcmp, CN)                                                                                                     \
  asm volatile("s_cmp_" flagcmp "_u32 %8, 3\n\ts_cbranch_scc1 1f\n\t"                                                          \
               "buffer_load_dwordx4 %0, %9, %10, %11 offen offset:0\n\tbuffer_load_dwordx4 %1, %9, %10, %11 offen offset:32\n\t"   \
               "buffer_load_dwordx4 %2, %9, %10, %11 offen offset:64\n\tbuffer_load_dwordx4 %3, %9, %10, %11 offen offset:96\n\t"  \
               "buffer_load_dwordx4 %4, %9, %10, %11 offen offset:128\n\tbuffer_load_dwordx4 %5, %9, %10, %11 offen offset:160\n\t" \
               "buffer_load_dwordx4 %6, %9, %10, %11 offen offset:192\n\tbuffer_load_dwordx4 %7, %9, %10, %11 offen offset:224\n1:" \
               : CN(c.q[0]), CN(c.q[1]), CN(c.q[2]), CN(c.q[3]), CN(c.q[4]), CN(c.q[5]), CN(c.q[6]), CN(c.q[7])  \
               : "s"(c.isA), "v"(qvoff), "s"(srdQ), "s"(qsoff) : "memory", "scc")
  QLOAD_IF("gt", "=&v");
#pragma unroll
  for (int t = 0; t < 10; ++t) DMA(ldsb + wid * 1024u + t * 8192u, ksrc, srdK, t * 8192u);
#pragma unroll
  for (int t = 0; t < 10; ++t) DMA(ldsb + VOFF + wid * 1024u + t * 8192u, vsrc, srdV, t * 8192u);
  QLOAD_IF("le", "+v");

  {
    const unsigned kk = r32 >> 2, rho = kk & 3u;
    const unsigned kb_ = ldsb + kk * 1024u + (r32 & 3u) * 16u + ((hi ^ (rho & 1u)) * 64u);
    c.kb[0][0] = kb_ + ((rho >> 1) * 128u); c.kb[0][1] = kb_ + (((rho >> 1) ^ 1u) * 128u);
    c.kb[1][0] = c.kb[0][0] + HALF_BYTES; c.kb[1][1] = c.kb[0][1] + HALF_BYTES;
    c.vb[0] = ldsb + VOFF + hi * 1024u + ((lane >> 2) & 3u) * 64u + ((lane >> 4) & 1u) * 32u + (lane & 3u) * 8u;
    c.vb[1] = c.vb[0] + HALF_BYTES;
    asm volatile("" : "+v"(c.kb[0][0]), "+v"(c.kb[0][1]), "+v"(c.kb[1][0]), "+v"(c.kb[1][1]), "+v"(c.vb[0]), "+v"(c.vb[1]));
  }
  asm volatile("s_cmp_gt_u32 %9, 3\n\ts_cbranch_scc1 1f\n\t"
               "s_waitcnt vmcnt(15)\n\ts_barrier\n\ts_branch 2f\n"
               "1:\n\ts_waitcnt vmcnt(23)\n\ts_barrier\n\ts_waitcnt vmcnt(18)\n\ts_barrier\n\ts_waitcnt vmcnt(13)\n\ts_barrier\n\t"
               "s_waitcnt vmcnt(8)\n\ts_barrier\n\ts_waitcnt vmcnt(0)\n2:"
               : "+v"(c.q[0]), "+v"(c.q[1]), "+v"(c.q[2]), "+v"(c.q[3]), "+v"(c.q[4]), "+v"(c.q[5]), "+v"(c.q[6]), "+v"(c.q[7]), "+v"(pfd)
               : "s"(c.isA) : "memory", "scc");

  c.l = 0.f; c.mx = -3.0e38f;
  c.fr[0] = frag<0>(c); c.fr[1] = frag<1>(c); c.fr[2] = frag<2>(c);
  GAP();
  steps(c, make_iseq<124>{});
  const unsigned q2off = (8u + ((wid >> 1) & 1u)) * 8192u;
  {
    const unsigned qsoff = q2off;
    QLOAD_IF("gt", "=&v");
  }
  GAP();
  steps_from<124>(c, make_iseq<36>{});
  Epi ep; ep.srdO = srdO; ep.voff = qvoff; ep.soff = qsoff; ep.il = __builtin_amdgcn_rcpf(xhalf_sum(c.l));

  if (wid < 4) {
    if (wid & 1u) { c.kb[0][0] = c.kb[1][0]; c.kb[0][1] = c.kb[1][1]; c.vb[0] = c.vb[1]; }
    asm volatile("" : "+v"(c.kb[0][0]), "+v"(c.kb[0][1]), "+v"(c.vb[0]));
    QWAIT(c.q, 0);
    c.mx = -3.0e38f; c.l = 0.f;
    c.fr[0] = ufrag<0>(c); c.fr[1] = ufrag<1>(c); c.fr[2] = ufrag<2>(c);
    GAP();
    usteps(c, ep, make_iseq<80>{});
    const float m = c.mx;
    const float l = xhalf_sum(c.l);
    asm volatile("s_barrier" ::: "memory");
    const unsigned xw = ldsb + wid * 16384u + lane * 16u, xr = ldsb + (wid ^ 1u) * 16384u + lane * 16u;
    f32x16 k0, k1, s0, s1;
    if (wid & 1u) { k0 = c.O[2]; k1 = c.O[3]; s0 = c.O[0]; s1 = c.O[1]; } else { k0 = c.O[0]; k1 = c.O[1]; s0 = c.O[2]; s1 = c.O[3]; }
#pragma unroll
    for (int j = 0; j < 4; ++j) {
      *(LDS3 f32x4*)(xw + j * 1024) = f32x4{s0[4 * j], s0[4 * j + 1], s0[4 * j + 2], s0[4 * j + 3]};
      *(LDS3 f32x4*)(xw + 4096 + j * 1024) = f32x4{s1[4 * j], s1[4 * j + 1], s1[4 * j + 2], s1[4 * j + 3]};
    }
    *(LDS3 f32x4*)(xw + 8192) = f32x4{m, l, 0.f, 0.f};
    asm volatile("s_waitcnt lgkmcnt(0)\n\ts_barrier" ::: "memory");
    const f32x4 ml = *(const LDS3 f32x4*)(xr + 8192);
    const float mm = __builtin_fmaxf(m, ml[0]);
    const float fa = __builtin_amdgcn_exp2f((m - mm) * CEXP), fb = __builtin_amdgcn_exp2f((ml[0] - mm) * CEXP);
    const float il = __builtin_amdgcn_rcpf(l * fa + ml[1] * fb);
    const float ra = fa * il, rb = fb * il;
#pragma unroll
    for (int j = 0; j < 4; ++j) {
      const f32x4 a = *(const LDS3 f32x4*)(xr + j * 1024), b = *(const LDS3 f32x4*)(xr + 4096 + j * 1024);
#pragma unroll
      for (int e = 0; e < 4; ++e) { k0[4 * j + e] = k0[4 * j + e] * ra + a[e] * rb; k1[4 * j + e] = k1[4 * j + e] * ra + b[e] * rb; }
    }
    const unsigned o2off = q2off + (wid & 1u) * 128u;
    store_db(k0, 1.f, srdO, qvoff, o2off, 0);
    store_db(k1, 1.f, srdO, qvoff, o2off, 1);
  } else {
#pragma unroll
    for (int db = 0; db < 4; ++db) store_db(c.O[db], ep.il, srdO, qvoff, qsoff, db);
    asm volatile("s_barrier\n\ts_barrier" ::: "memory");
  }
  asm volatile(".Lgqa_pf_end:" ::: "memory");
}

extern "C" void kernel_launch(void* const* d_in, const int* in_sizes, int n_in,
                              void* d_out, int out_size, void* d_ws, size_t ws_size,
                              hipStream_t stream) {
  gqa_attn_kernel<<<dim3(256), dim3(512), 0, stream>>>((const unsigned short*)d_in[0], (const unsigned short*)d_in[1],
                                                      (const unsigned short*)d_in[2], (unsigned short*)d_out);
}
```

```cpp
#include <hip/hip_runtime.h>
#include <hip/hip_bf16.h>
#include <utility>

typedef __attribute__((ext_vector_type(8))) __bf16 bf16x8;
typedef __attribute__((ext_vector_type(4))) __bf16 bf16x4;
typedef __attribute__((ext_vector_type(2))) __bf16 bf16x2;
typedef __attribute__((ext_vector_type(16))) float f32x16;
typedef __attribute__((ext_vector_type(4))) float f32x4;
typedef __attribute__((ext_vector_type(2))) float f32x2;
typedef __attribute__((ext_vector_type(4))) unsigned u32x4;
typedef __attribute__((ext_vector_type(2))) unsigned u32x2;
#define LDS3 __attribute__((address_space(3)))
#define DI __device__ __forceinline__
#define GAP() __builtin_amdgcn_sched_barrier(0)
template <int... Is> using iseq = std::integer_sequence<int, Is...>;
template <int N> using make_iseq = std::make_integer_sequence<int, N>;

constexpr int SEQ = 320, DH = 128;
constexpr unsigned HEAD_BYTES = SEQ * DH * 2;
constexpr unsigned HALF_BYTES = HEAD_BYTES / 2;
constexpr unsigned VOFF = HEAD_BYTES;
constexpr float CEXP = 0.088388347648318440f * 1.4426950408889634f;
constexpr int PD = 3;

#define ST_AUX 0

DI unsigned cvtpk(float lo, float hi) {
  f32x2 v = {lo, hi}; bf16x2 b = __builtin_convertvector(v, bf16x2); return __builtin_bit_cast(unsigned, b);
}
DI float max3f(float a, float b, float c) { return __builtin_fmaxf(__builtin_fmaxf(a, b), c); }
DI float xhalf_max(float x) {
  auto r = __builtin_amdgcn_permlane32_swap(__float_as_uint(x), __float_as_uint(x), false, false);
  return __builtin_fmaxf(__uint_as_float(r[0]), __uint_as_float(r[1]));
}
DI float xhalf_sum(float x) {
  auto r = __builtin_amdgcn_permlane32_swap(__float_as_uint(x), __float_as_uint(x), false, false);
  return __uint_as_float(r[0]) + __uint_as_float(r[1]);
}
DI bf16x8 ldK(unsigned base, int off) { return *(const LDS3 bf16x8*)(base + off); }
DI bf16x8 ldV(unsigned base, int off) {
  bf16x4 a = __builtin_amdgcn_ds_read_tr16_b64_v4bf16((LDS3 bf16x4*)(base + off));
  bf16x4 b = __builtin_amdgcn_ds_read_tr16_b64_v4bf16((LDS3 bf16x4*)(base + off + 2048));
  return __builtin_shufflevector(a, b, 0, 1, 2, 3, 4, 5, 6, 7);
}

#define STAMP(i) do {} while (0)
struct Ctx {
  unsigned kb[2][2];
  unsigned vb[2];
  bf16x8 q[8];
  bf16x8 fr[PD + 1];
  f32x16 S[2][5];
  u32x4 P[2][10];
  f32x16 O[4];
  float mx, mc[2], m0, alpha, l;
  unsigned isA;
};

template <int G> DI bf16x8 frag(const Ctx& c) {
  if constexpr (G < 80) { constexpr int hf = G / 40, i = G % 40, t = i >> 3, s = i & 7; return ldK(c.kb[hf][s & 1], t * 8192 + (s >> 1) * 256); }
  else { constexpr int hf = (G - 80) / 40, i = (G - 80) % 40, ks = i >> 2, db = i & 3; return ldV(c.vb[hf], ks * 4096 + db * 256); }
}
template <int HF, int E0, int E1> DI void sm_range(Ctx& c) {
#pragma unroll
  for (int e = E0; e < E1; e += 2) {
    const float a = __builtin_amdgcn_exp2f(__builtin_fmaf(c.S[HF][e >> 4][e & 15], CEXP, c.mc[HF]));
    const float b = __builtin_amdgcn_exp2f(__builtin_fmaf(c.S[HF][e >> 4][(e & 15) + 1], CEXP, c.mc[HF]));
    c.l += a; c.l += b;
    if ((e & 7) == 0) asm volatile("" : "=v"(c.P[HF][e >> 3]));
    c.P[HF][e >> 3][(e >> 1) & 3] = cvtpk(a, b);
  }
}
template <int HF, int T, int I0, int I1> DI void max_range(Ctx& c) {
#pragma unroll
  for (int i = I0; i < I1; ++i) c.mx = max3f(c.mx, c.S[HF][T][2 * i], c.S[HF][T][2 * i + 1]);
}
#define CONDBAR(flag, N) asm volatile("s_cmp_gt_u32 %0, 3\n\ts_cbranch_scc1 1f\n\ts_waitcnt vmcnt(" #N ")\n\ts_barrier\n1:" :: "s"(flag) : "memory", "scc")

template <int G> DI void step(Ctx& c) {
  if constexpr (G == 40) STAMP(3);
  if constexpr (G == 80) STAMP(4);
  if constexpr (G == 120) STAMP(5);
  if constexpr (G + PD == 40) CONDBAR(c.isA, 10);
  if constexpr (G + PD == 80) CONDBAR(c.isA, 5);
  if constexpr (G + PD == 120) CONDBAR(c.isA, 0);
  if constexpr (G + PD < 160) c.fr[(G + PD) % (PD + 1)] = frag<G + PD>(c);
  const bf16x8 f = c.fr[G % (PD + 1)];
  if constexpr (G < 80) {
    constexpr int hf = G / 40, i = G % 40, t = i >> 3, s = i & 7;
    if constexpr (s == 0) c.S[hf][t] = __builtin_amdgcn_mfma_f32_32x32x16_bf16(f, c.q[s], f32x16{}, 0, 0, 0);
    else c.S[hf][t] = __builtin_amdgcn_mfma_f32_32x32x16_bf16(f, c.q[s], c.S[hf][t], 0, 0, 0);
    if constexpr (t > 0 && s >= 2 && s < 6) max_range<hf, t - 1, 2 * (s - 2), 2 * (s - 2) + 2>(c);
  } else {
    constexpr int hf = (G - 80) / 40, i = (G - 80) % 40, ks = i >> 2, db = i & 3;
    if constexpr (hf == 1 && ks == 0) {
#pragma unroll
      for (int e = 0; e < 16; ++e) c.O[db][e] *= c.alpha;
    }
    if constexpr (G < 84) c.O[db] = __builtin_amdgcn_mfma_f32_32x32x16_bf16(f, __builtin_bit_cast(bf16x8, c.P[hf][ks]), f32x16{}, 0, 0, 0);
    else c.O[db] = __builtin_amdgcn_mfma_f32_32x32x16_bf16(f, __builtin_bit_cast(bf16x8, c.P[hf][ks]), c.O[db], 0, 0, 0);
  }
  if constexpr (G >= 40 && G < 120) {
    constexpr int hf = G / 40 - 1, i = G % 40;
    if constexpr (i == 1) max_range<hf, 4, 0, 8>(c);
    if constexpr (i == 1) {
      const float m = xhalf_max(c.mx);
      c.mx = m; c.mc[hf] = -m * CEXP;
      if constexpr (hf == 0) c.m0 = m;
      else { c.alpha = __builtin_amdgcn_exp2f((c.m0 - m) * CEXP); c.l *= c.alpha; }
    }
    if constexpr (i >= 2) {
      constexpr int e0 = 2 * ((40 * (i - 2)) / 38), e1 = 2 * ((40 * (i - 1)) / 38);
      sm_range<hf, e0, e1>(c);
      if constexpr (((e1 - 1) >> 3) != (e0 >> 3)) asm volatile("" : "+v"(c.l), "+v"(c.P[hf][(e1 - 1) >> 3]), "+v"(c.P[hf][e0 >> 3]));
      else asm volatile("" : "+v"(c.l), "+v"(c.P[hf][e0 >> 3]));
    }
  }
  if constexpr (G < 80) asm volatile("" : "+v"(c.mx));
  GAP();
}
template <int... Gs> DI void steps(Ctx& c, iseq<Gs...>) { (step<Gs>(c), ...); }
template <int B, int... Gs> DI void steps_from(Ctx& c, iseq<Gs...>) { (step<B + Gs>(c), ...); }

template <int G2> DI void store_g(const f32x16& o, float sc, __amdgpu_buffer_rsrc_t srdO, unsigned voff, unsigned soff, int db) {
  constexpr int g = 2 * G2;
  unsigned a0 = cvtpk(o[4 * g + 0] * sc, o[4 * g + 1] * sc), a1 = cvtpk(o[4 * g + 2] * sc, o[4 * g + 3] * sc);
  unsigned b0 = cvtpk(o[4 * g + 4] * sc, o[4 * g + 5] * sc), b1 = cvtpk(o[4 * g + 6] * sc, o[4 * g + 7] * sc);
  auto r0 = __builtin_amdgcn_permlane32_swap(a0, b0, false, false);
  auto r1 = __builtin_amdgcn_permlane32_swap(a1, b1, false, false);
  u32x4 w = {r0[0], r1[0], r0[1], r1[1]};
  __builtin_amdgcn_raw_buffer_store_b128(w, srdO, voff + soff + db * 64 + g * 16, 0, ST_AUX);
}
DI void store_db(const f32x16& o, float sc, __amdgpu_buffer_rsrc_t srdO, unsigned voff, unsigned soff, int db) {
  store_g<0>(o, sc, srdO, voff, soff, db); store_g<1>(o, sc, srdO, voff, soff, db);
}

struct Epi { __amdgpu_buffer_rsrc_t srdO; unsigned voff, soff; float il; };
template <int G> DI bf16x8 ufrag(const Ctx& c) {
  if constexpr (G < 40) { constexpr int t = G >> 3, s = G & 7; return ldK(c.kb[0][s & 1], t * 8192 + (s >> 1) * 256); }
  else { constexpr int i = G - 40, ks = i >> 2, db = i & 3; return ldV(c.vb[0], ks * 4096 + db * 256); }
}
template <int G> DI void ustep(Ctx& c, const Epi& ep) {
  if constexpr (G == 40) STAMP(8);
  if constexpr (G + PD < 80) c.fr[(G + PD) % (PD + 1)] = ufrag<G + PD>(c);
  const bf16x8 f = c.fr[G % (PD + 1)];
  if constexpr (G < 40) {
    constexpr int t = G >> 3, s = G & 7;
    if constexpr (s == 0) c.S[0][t] = __builtin_amdgcn_mfma_f32_32x32x16_bf16(f, c.q[s], f32x16{}, 0, 0, 0);
    else c.S[0][t] = __builtin_amdgcn_mfma_f32_32x32x16_bf16(f, c.q[s], c.S[0][t], 0, 0, 0);
    if constexpr (t > 0 && s >= 2 && s < 6) max_range<0, t - 1, 2 * (s - 2), 2 * (s - 2) + 2>(c);
    if constexpr ((G & 3) == 1 && G < 32) store_g<(G >> 2) & 1>(c.O[G >> 3], ep.il, ep.srdO, ep.voff, ep.soff, G >> 3);
    asm volatile("" : "+v"(c.mx));
    if constexpr (G == 39) {
      max_range<0, 4, 0, 8>(c);
      const float m = xhalf_max(c.mx);
      c.mx = m; c.mc[0] = -m * CEXP;
      sm_range<0, 0, 8>(c);
    }
  } else {
    constexpr int i = G - 40, ks = i >> 2, db = i & 3;
    if constexpr (ks == 0) c.O[db] = __builtin_amdgcn_mfma_f32_32x32x16_bf16(f, __builtin_bit_cast(bf16x8, c.P[0][ks]), f32x16{}, 0, 0, 0);
    else c.O[db] = __builtin_amdgcn_mfma_f32_32x32x16_bf16(f, __builtin_bit_cast(bf16x8, c.P[0][ks]), c.O[db], 0, 0, 0);
    if constexpr (ks < 9) {
      sm_range<0, 8 + 2 * i, 8 + 2 * i + 2>(c);
      asm volatile("" : "+v"(c.l), "+v"(c.P[0][ks + 1]));
    }
  }
  GAP();
}
template <int... Gs> DI void usteps(Ctx& c, const Epi& ep, iseq<Gs...>) { (ustep<Gs>(c, ep), ...); }

#define QLOAD(qq, soff)                                                                                      \
  do {                                                                                                       \
    _Pragma("unroll") for (int s_ = 0; s_ < 8; ++s_)                                                         \
      asm volatile("buffer_load_dwordx4 %0, %1, %2, %3 offen offset:%4"                                      \
                   : "=v"(qq[s_]) : "v"(qvoff), "s"(srdQ), "s"(soff), "i"(32 * s_) : "memory");              \
  } while (0)
#define QWAIT(qq, N)                                                                                         \
  asm volatile("s_waitcnt vmcnt(" #N ")"                                                                     \
               : "+v"(qq[0]), "+v"(qq[1]), "+v"(qq[2]), "+v"(qq[3]), "+v"(qq[4]), "+v"(qq[5]), "+v"(qq[6]), "+v"(qq[7]) :: "memory")
#define DMA(m0v, voff, srd, soff)                                                                            \
  asm volatile("s_mov_b32 m0, %0\n\ts_nop 0\n\tbuffer_load_dwordx4 %1, %2, %3 offen lds"                     \
               :: "s"(m0v), "v"(voff), "s"(srd), "s"(soff) : "memory")

__global__ __launch_bounds__(512, 2)
void gqa_attn_kernel(const unsigned short* __restrict__ Q, const unsigned short* __restrict__ K,
                     const unsigned short* __restrict__ V, unsigned short* __restrict__ O
                     ) {
  __shared__ __attribute__((aligned(16))) char lds[2 * HEAD_BYTES];
  const unsigned ldsb = (unsigned)(size_t)(LDS3 char*)lds;
  const unsigned lane = threadIdx.x & 63u, r32 = lane & 31u, hi = lane >> 5;
  unsigned wid = __builtin_amdgcn_readfirstlane(threadIdx.x >> 6);
  asm volatile("s_nop 4" : "+s"(wid));
  const unsigned bid = blockIdx.x;
  const unsigned grp = (bid & 7u) + 8u * (bid >> 5);
  const unsigned head = grp * 4u + ((bid >> 3) & 3u);
  Ctx c;
  c.isA = wid;
  STAMP(0);

  unsigned pfd = 0;
  if (wid == 7) {
    unsigned long long pc; unsigned len;
    asm volatile("s_getpc_b64 %0\n.Lgqa_pf_start:\n\ts_mov_b32 %1, .Lgqa_pf_end-.Lgqa_pf_start" : "=s"(pc), "=s"(len));
    for (unsigned off = 0; off < len; off += 4096) {
      unsigned o = off + lane * 64u; o = o < len - 4u ? o : len - 4u;
      asm volatile("global_load_dword %0, %1, %2" : "+v"(pfd) : "v"(o), "s"(pc) : "memory");
    }
  }

  __amdgpu_buffer_rsrc_t srdQ = __builtin_amdgcn_make_buffer_rsrc((void*)((const char*)Q + (size_t)head * HEAD_BYTES), (short)0, (int)HEAD_BYTES, 0x00020000);
  __amdgpu_buffer_rsrc_t srdK = __builtin_amdgcn_make_buffer_rsrc((void*)((const char*)K + (size_t)grp * HEAD_BYTES), (short)0, (int)HEAD_BYTES, 0x00020000);
  __amdgpu_buffer_rsrc_t srdV = __builtin_amdgcn_make_buffer_rsrc((void*)((const char*)V + (size_t)grp * HEAD_BYTES), (short)0, (int)HEAD_BYTES, 0x00020000);
  __amdgpu_buffer_rsrc_t srdO = __builtin_amdgcn_make_buffer_rsrc((void*)((char*)O + (size_t)head * HEAD_BYTES), (short)0, (int)HEAD_BYTES, 0x00020000);

  const unsigned qvoff = r32 * 256u + hi * 16u;
  const unsigned ksrc = wid * 1024u + (lane & 3u) * 256u + (((lane >> 2) ^ (wid & 3u)) * 16u);
  const unsigned vsrc = wid * 1024u + ((lane >> 2) & 3u) * 256u + (((lane >> 4) * 4u + (lane & 3u)) * 16u);
  const unsigned qsoff = wid * 8192u;

#define QLOAD_IF(flagcmp, CN)                                                                                                     \
  asm volatile("s_cmp_" flagcmp "_u32 %8, 3\n\ts_cbranch_scc1 1f\n\t"                                                          \
               "buffer_load_dwordx4 %0, %9, %10, %11 offen offset:0\n\tbuffer_load_dwordx4 %1, %9, %10, %11 offen offset:32\n\t"   \
               "buffer_load_dwordx4 %2, %9, %10, %11 offen offset:64\n\tbuffer_load_dwordx4 %3, %9, %10, %11 offen offset:96\n\t"  \
               "buffer_load_dwordx4 %4, %9, %10, %11 offen offset:128\n\tbuffer_load_dwordx4 %5, %9, %10, %11 offen offset:160\n\t" \
               "buffer_load_dwordx4 %6, %9, %10, %11 offen offset:192\n\tbuffer_load_dwordx4 %7, %9, %10, %11 offen offset:224\n1:" \
               : CN(c.q[0]), CN(c.q[1]), CN(c.q[2]), CN(c.q[3]), CN(c.q[4]), CN(c.q[5]), CN(c.q[6]), CN(c.q[7])  \
               : "s"(c.isA), "v"(qvoff), "s"(srdQ), "s"(qsoff) : "memory", "scc")
  QLOAD_IF("gt", "=&v");
#pragma unroll
  for (int t = 0; t < 10; ++t) DMA(ldsb + wid * 1024u + t * 8192u, ksrc, srdK, t * 8192u);
#pragma unroll
  for (int t = 0; t < 10; ++t) DMA(ldsb + VOFF + wid * 1024u + t * 8192u, vsrc, srdV, t * 8192u);
  QLOAD_IF("le", "+v");

  {
    const unsigned kk = r32 >> 2, rho = kk & 3u;
    const unsigned kb_ = ldsb + kk * 1024u + (r32 & 3u) * 16u + ((hi ^ (rho & 1u)) * 64u);
    c.kb[0][0] = kb_ + ((rho >> 1) * 128u); c.kb[0][1] = kb_ + (((rho >> 1) ^ 1u) * 128u);
    c.kb[1][0] = c.kb[0][0] + HALF_BYTES; c.kb[1][1] = c.kb[0][1] + HALF_BYTES;
    c.vb[0] = ldsb + VOFF + hi * 1024u + ((lane >> 2) & 3u) * 64u + ((lane >> 4) & 1u) * 32u + (lane & 3u) * 8u;
    c.vb[1] = c.vb[0] + HALF_BYTES;
    asm volatile("" : "+v"(c.kb[0][0]), "+v"(c.kb[0][1]), "+v"(c.kb[1][0]), "+v"(c.kb[1][1]), "+v"(c.vb[0]), "+v"(c.vb[1]));
  }
  STAMP(1);
  asm volatile("s_cmp_gt_u32 %9, 3\n\ts_cbranch_scc1 1f\n\t"
               "s_waitcnt vmcnt(15)\n\ts_barrier\n\ts_branch 2f\n"
               "1:\n\ts_waitcnt vmcnt(23)\n\ts_barrier\n\ts_waitcnt vmcnt(18)\n\ts_barrier\n\ts_waitcnt vmcnt(13)\n\ts_barrier\n\t"
               "s_waitcnt vmcnt(8)\n\ts_barrier\n\ts_waitcnt vmcnt(0)\n2:"
               : "+v"(c.q[0]), "+v"(c.q[1]), "+v"(c.q[2]), "+v"(c.q[3]), "+v"(c.q[4]), "+v"(c.q[5]), "+v"(c.q[6]), "+v"(c.q[7]), "+v"(pfd)
               : "s"(c.isA) : "memory", "scc");

  STAMP(2);
  c.l = 0.f; c.mx = -3.0e38f;
  c.fr[0] = frag<0>(c); c.fr[1] = frag<1>(c); c.fr[2] = frag<2>(c);
  GAP();
  steps(c, make_iseq<124>{});
  const unsigned q2off = (8u + ((wid >> 1) & 1u)) * 8192u;
  {
    const unsigned qsoff = q2off;
    QLOAD_IF("gt", "=&v");
  }
  GAP();
  steps_from<124>(c, make_iseq<36>{});
  STAMP(6);
  Epi ep; ep.srdO = srdO; ep.voff = qvoff; ep.soff = qsoff; ep.il = __builtin_amdgcn_rcpf(xhalf_sum(c.l));

  if (wid < 4) {
    if (wid & 1u) { c.kb[0][0] = c.kb[1][0]; c.kb[0][1] = c.kb[1][1]; c.vb[0] = c.vb[1]; }
    asm volatile("" : "+v"(c.kb[0][0]), "+v"(c.kb[0][1]), "+v"(c.vb[0]));
    QWAIT(c.q, 0);
    STAMP(7);
    c.mx = -3.0e38f; c.l = 0.f;
    c.fr[0] = ufrag<0>(c); c.fr[1] = ufrag<1>(c); c.fr[2] = ufrag<2>(c);
    GAP();
    usteps(c, ep, make_iseq<80>{});
    STAMP(9);
    const float m = c.mx;
    const float l = xhalf_sum(c.l);
    asm volatile("s_barrier" ::: "memory");
    const unsigned xw = ldsb + wid * 16384u + lane * 16u, xr = ldsb + (wid ^ 1u) * 16384u + lane * 16u;
    f32x16 k0, k1, s0, s1;
    if (wid & 1u) { k0 = c.O[2]; k1 = c.O[3]; s0 = c.O[0]; s1 = c.O[1]; } else { k0 = c.O[0]; k1 = c.O[1]; s0 = c.O[2]; s1 = c.O[3]; }
#pragma unroll
    for (int j = 0; j < 4; ++j) {
      *(LDS3 f32x4*)(xw + j * 1024) = f32x4{s0[4 * j], s0[4 * j + 1], s0[4 * j + 2], s0[4 * j + 3]};
      *(LDS3 f32x4*)(xw + 4096 + j * 1024) = f32x4{s1[4 * j], s1[4 * j + 1], s1[4 * j + 2], s1[4 * j + 3]};
    }
    *(LDS3 f32x4*)(xw + 8192) = f32x4{m, l, 0.f, 0.f};
    asm volatile("s_waitcnt lgkmcnt(0)\n\ts_barrier" ::: "memory");
    const f32x4 ml = *(const LDS3 f32x4*)(xr + 8192);
    const float mm = __builtin_fmaxf(m, ml[0]);
    const float fa = __builtin_amdgcn_exp2f((m - mm) * CEXP), fb = __builtin_amdgcn_exp2f((ml[0] - mm) * CEXP);
    const float il = __builtin_amdgcn_rcpf(l * fa + ml[1] * fb);
    const float ra = fa * il, rb = fb * il;
#pragma unroll
    for (int j = 0; j < 4; ++j) {
      const f32x4 a = *(const LDS3 f32x4*)(xr + j * 1024), b = *(const LDS3 f32x4*)(xr + 4096 + j * 1024);
#pragma unroll
      for (int e = 0; e < 4; ++e) { k0[4 * j + e] = k0[4 * j + e] * ra + a[e] * rb; k1[4 * j + e] = k1[4 * j + e] * ra + b[e] * rb; }
    }
    const unsigned o2off = q2off + (wid & 1u) * 128u;
    store_db(k0, 1.f, srdO, qvoff, o2off, 0);
    store_db(k1, 1.f, srdO, qvoff, o2off, 1);
  } else {
#pragma unroll
    for (int db = 0; db < 4; ++db) store_db(c.O[db], ep.il, srdO, qvoff, qsoff, db);
    asm volatile("s_barrier\n\ts_barrier" ::: "memory");
  }
  STAMP(10);
  asm volatile(".Lgqa_pf_end:" ::: "memory");
}

extern "C" void kernel_launch(void* const* d_in, const int* in_sizes, int n_in,
                              void* d_out, int out_size, void* d_ws, size_t ws_size,
                              hipStream_t stream) {
  gqa_attn_kernel<<<dim3(256), dim3(512), 0, stream>>>((const unsigned short*)d_in[0], (const unsigned short*)d_in[1],
                                                      (const unsigned short*)d_in[2], (unsigned short*)d_out
                                                      );
}
```

```cpp
#include <hip/hip_runtime.h>
#include <hip/hip_bf16.h>

#define LDS3 __attribute__((address_space(3)))

__global__ __launch_bounds__(512, 2)
void gqa_attn_kernel(const unsigned short* __restrict__ Q, const unsigned short* __restrict__ K,
                     const unsigned short* __restrict__ V, unsigned short* __restrict__ O, unsigned* __restrict__ WS) {
  __shared__ __attribute__((aligned(16))) char lds[2 * 320 * 128 * 2];
  const unsigned ldsb = (unsigned)(size_t)(LDS3 char*)lds;
  asm volatile(
    "  s_load_dwordx8 s[40:47], %[ka], 0x0\n"
    "  s_load_dwordx2 s[48:49], %[ka], 0x20\n"
    "  v_readfirstlane_b32 s26, %[tid]\n"
    "  v_and_b32 v14, 63, %[tid]\n"
    "  s_mov_b32 s37, %[bid]\n"
    "  s_mov_b32 s38, %[ldsb]\n"
    "  s_lshr_b32 s26, s26, 6\n"
    "  s_lshr_b32 s50, s37, 5\n"
    "  s_lshl_b32 s50, s50, 3\n"
    "  s_and_b32 s51, s37, 7\n"
    "  s_add_u32 s50, s50, s51\n"
    "  s_bfe_u32 s51, s37, 0x20003\n"
    "  s_lshl_b32 s52, s50, 2\n"
    "  s_add_u32 s51, s51, s52\n"
    "  s_mul_i32 s52, s50, 0x14000\n"
    "  s_mul_i32 s53, s51, 0x14000\n"
    "  s_and_b32 s54, s26, 3\n"
    "  s_and_b32 s55, s26, 1\n"
    "  s_lshr_b32 s56, s54, 1\n"
    "  s_lshl_b32 s27, s26, 13\n"
    "  v_lshlrev_b32 v10, 8, v14\n"
    "  v_and_b32 v10, 0x1c00, v10\n"
    "  v_lshrrev_b32 v15, 5, v14\n"
    "  v_lshl_or_b32 v10, v15, 6, v10\n"
    "  v_and_b32 v15, 3, v14\n"
    "  v_lshl_or_b32 v10, v15, 4, v10\n"
    "  s_cmp_gt_u32 s26, 3\n"
    "  s_cbranch_scc1 .Lgqa_bpro\n"
    "  s_setprio 3\n"
    "  s_waitcnt lgkmcnt(0)\n"
    "  s_add_u32 s4, s40, s53\n"
    "  s_addc_u32 s5, s41, 0\n"
    "  s_and_b32 s5, s5, 0xffff\n"
    "  s_mov_b32 s6, 0x14000\n"
    "  s_mov_b32 s7, 0x20000\n"
    "  buffer_load_dwordx4 v[128:131], v10, s[4:7], s27 offen offset:0 nt\n"
    "  buffer_load_dwordx4 v[132:135], v10, s[4:7], s27 offen offset:128 nt\n"
    "  buffer_load_dwordx4 v[136:139], v10, s[4:7], s27 offen offset:256 nt\n"
    "  buffer_load_dwordx4 v[140:143], v10, s[4:7], s27 offen offset:384 nt\n"
    "  buffer_load_dwordx4 v[144:147], v10, s[4:7], s27 offen offset:512 nt\n"
    "  buffer_load_dwordx4 v[148:151], v10, s[4:7], s27 offen offset:640 nt\n"
    "  buffer_load_dwordx4 v[152:155], v10, s[4:7], s27 offen offset:768 nt\n"
    "  buffer_load_dwordx4 v[156:159], v10, s[4:7], s27 offen offset:896 nt\n"
    "  v_lshlrev_b32 v13, 2, v14\n"
    "  s_add_u32 s16, s46, s53\n"
    "  s_addc_u32 s17, s47, 0\n"
    "  s_and_b32 s17, s17, 0xffff\n"
    "  s_mov_b32 s18, 0x14000\n"
    "  s_mov_b32 s19, 0x20000\n"
    "  s_lshl_b32 s57, s37, 7\n"
    "  s_add_u32 s24, s48, s57\n"
    "  s_addc_u32 s25, s49, 0\n"
    "  s_mov_b32 s20, s24\n"
    "  s_and_b32 s21, s25, 0xffff\n"
    "  s_mov_b32 s22, 64\n"
    "  s_mov_b32 s23, 0x20000\n"
    "  v_lshlrev_b32 v1, 8, v14\n"
    "  v_and_b32 v1, 0x1800, v1\n"
    "  v_lshlrev_b32 v15, 7, v14\n"
    "  v_and_b32 v15, 0x380, v15\n"
    "  v_or_b32 v1, v1, v15\n"
    "  v_lshrrev_b32 v15, 3, v14\n"
    "  v_lshrrev_b32 v2, 1, v14\n"
    "  v_xor_b32 v15, v15, v2\n"
    "  v_and_b32 v15, 4, v15\n"
    "  v_and_b32 v2, 3, v2\n"
    "  v_or_b32 v15, v15, v2\n"
    "  v_lshl_or_b32 v1, v15, 4, v1\n"
    "  v_add_u32 v1, s38, v1\n"
    "  v_xor_b32 v2, 16, v1\n"
    "  v_xor_b32 v3, 32, v1\n"
    "  v_xor_b32 v4, 48, v1\n"
    "  v_lshrrev_b32 v15, 5, v14\n"
    "  v_lshlrev_b32 v5, 9, v15\n"
    "  v_lshlrev_b32 v15, 5, v14\n"
    "  v_and_b32 v15, 0x180, v15\n"
    "  v_or_b32 v5, v5, v15\n"
    "  v_lshlrev_b32 v15, 6, v14\n"
    "  v_and_b32 v15, 64, v15\n"
    "  v_or_b32 v5, v5, v15\n"
    "  v_and_b32 v15, 16, v14\n"
    "  v_or_b32 v5, v5, v15\n"
    "  v_lshlrev_b32 v15, 2, v14\n"
    "  v_and_b32 v15, 40, v15\n"
    "  v_or_b32 v5, v5, v15\n"
    "  s_add_u32 s57, s38, 0x14000\n"
    "  v_add_u32 v5, s57, v5\n"
    "  v_xor_b32 v6, 32, v5\n"
    "  v_add_u32 v7, 0x400, v5\n"
    "  v_add_u32 v8, 0x400, v6\n"
    "  s_lshl_b32 s28, s56, 13\n"
    "  s_add_u32 s28, s28, 0x10000\n"
    "  s_lshl_b32 s57, s55, 7\n"
    "  s_add_u32 s29, s28, s57\n"
    "  s_lshl_b32 s42, s54, 2\n"
    "  s_lshl_b32 s43, s26, 14\n"
    "  s_add_u32 s43, s43, s38\n"
    "  s_cmp_eq_u32 s55, 0\n"
    "  s_cselect_b32 s31, 0xa000, 0\n"
    "  s_lshl_b32 s57, s55, 10\n"
    "  s_sub_u32 s40, s57, s31\n"
    "  s_add_u32 s57, s57, s31\n"
    "  s_sub_u32 s41, 0, s57\n"
    "  s_lshl_b32 s57, s55, 15\n"
    "  s_sub_u32 s44, 0x4000, s57\n"
    "  s_xor_b32 s45, s42, 4\n"
    "  s_add_u32 s45, s45, 16\n"
    "  s_mov_b32 s58, 0x55555555\n"
    "  s_mov_b32 s59, 0x55555555\n"
    "  s_mov_b32 s60, 0xaaaaaaaa\n"
    "  s_mov_b32 s61, 0xaaaaaaaa\n"
    "  s_mov_b32 s62, 0x33333333\n"
    "  s_mov_b32 s63, 0x33333333\n"
    "  s_mov_b32 s64, 0xcccccccc\n"
    "  s_mov_b32 s65, 0xcccccccc\n"
    "  v_mov_b32 v244, 0xff7fffff\n"
    "  v_mov_b32 v245, 0\n"
    "  v_mov_b32 v246, 0\n"
    "  v_mov_b32 v248, 0x3e0293ee\n"
    "  s_cmp_lg_u32 s26, 0\n"
    "  s_cbranch_scc1 .Lgqa_nz\n"
    "  buffer_store_dword v245, v13, s[20:23], 0 offen\n"
    ".Lgqa_nz:\n"
    "  s_waitcnt vmcnt(0)\n"
    "  s_mov_b64 vcc, s[58:59]\n"
    "  v_cndmask_b32_dpp v48, v136, v128, vcc quad_perm:[1,0,3,2] row_mask:0xf bank_mask:0xf\n"
    "  v_mov_b32 v255, v255\n"
    "  v_cndmask_b32_dpp v49, v137, v129, vcc quad_perm:[1,0,3,2] row_mask:0xf bank_mask:0xf\n"
    "  v_mov_b32 v255, v255\n"
    "  v_cndmask_b32_dpp v50, v138, v130, vcc quad_perm:[1,0,3,2] row_mask:0xf bank_mask:0xf\n"
    "  v_mov_b32 v255, v255\n"
    "  v_cndmask_b32_dpp v51, v139, v131, vcc quad_perm:[1,0,3,2] row_mask:0xf bank_mask:0xf\n"
    "  v_mov_b32 v255, v255\n"
    "  v_cndmask_b32_dpp v56, v152, v144, vcc quad_perm:[1,0,3,2] row_mask:0xf bank_mask:0xf\n"
    "  v_mov_b32 v255, v255\n"
    "  v_cndmask_b32_dpp v57, v153, v145, vcc quad_perm:[1,0,3,2] row_mask:0xf bank_mask:0xf\n"
    "  v_mov_b32 v255, v255\n"
    "  v_cndmask_b32_dpp v58, v154, v146, vcc quad_perm:[1,0,3,2] row_mask:0xf bank_mask:0xf\n"
    "  v_mov_b32 v255, v255\n"
    "  v_cndmask_b32_dpp v59, v155, v147, vcc quad_perm:[1,0,3,2] row_mask:0xf bank_mask:0xf\n"
    "  v_mov_b32 v255, v255\n"
    "  v_cndmask_b32_dpp v64, v140, v132, vcc quad_perm:[1,0,3,2] row_mask:0xf bank_mask:0xf\n"
    "  v_mov_b32 v255, v255\n"
    "  v_cndmask_b32_dpp v65, v141, v133, vcc quad_perm:[1,0,3,2] row_mask:0xf bank_mask:0xf\n"
    "  v_mov_b32 v255, v255\n"
    "  v_cndmask_b32_dpp v66, v142, v134, vcc quad_perm:[1,0,3,2] row_mask:0xf bank_mask:0xf\n"
    "  v_mov_b32 v255, v255\n"
    "  v_cndmask_b32_dpp v67, v143, v135, vcc quad_perm:[1,0,3,2] row_mask:0xf bank_mask:0xf\n"
    "  v_mov_b32 v255, v255\n"
    "  v_cndmask_b32_dpp v72, v156, v148, vcc quad_perm:[1,0,3,2] row_mask:0xf bank_mask:0xf\n"
    "  v_mov_b32 v255, v255\n"
    "  v_cndmask_b32_dpp v73, v157, v149, vcc quad_perm:[1,0,3,2] row_mask:0xf bank_mask:0xf\n"
    "  v_mov_b32 v255, v255\n"
    "  v_cndmask_b32_dpp v74, v158, v150, vcc quad_perm:[1,0,3,2] row_mask:0xf bank_mask:0xf\n"
    "  v_mov_b32 v255, v255\n"
    "  v_cndmask_b32_dpp v75, v159, v151, vcc quad_perm:[1,0,3,2] row_mask:0xf bank_mask:0xf\n"
    "  s_mov_b64 vcc, s[60:61]\n"
    "  v_cndmask_b32_dpp v52, v128, v136, vcc quad_perm:[1,0,3,2] row_mask:0xf bank_mask:0xf\n"
    "  v_mov_b32 v255, v255\n"
    "  v_cndmask_b32_dpp v53, v129, v137, vcc quad_perm:[1,0,3,2] row_mask:0xf bank_mask:0xf\n"
    "  v_mov_b32 v255, v255\n"
    "  v_cndmask_b32_dpp v54, v130, v138, vcc quad_perm:[1,0,3,2] row_mask:0xf bank_mask:0xf\n"
    "  v_mov_b32 v255, v255\n"
    "  v_cndmask_b32_dpp v55, v131, v139, vcc quad_perm:[1,0,3,2] row_mask:0xf bank_mask:0xf\n"
    "  v_mov_b32 v255, v255\n"
    "  v_cndmask_b32_dpp v60, v144, v152, vcc quad_perm:[1,0,3,2] row_mask:0xf bank_mask:0xf\n"
    "  v_mov_b32 v255, v255\n"
    "  v_cndmask_b32_dpp v61, v145, v153, vcc quad_perm:[1,0,3,2] row_mask:0xf bank_mask:0xf\n"
    "  v_mov_b32 v255, v255\n"
    "  v_cndmask_b32_dpp v62, v146, v154, vcc quad_perm:[1,0,3,2] row_mask:0xf bank_mask:0xf\n"
    "  v_mov_b32 v255, v255\n"
    "  v_cndmask_b32_dpp v63, v147, v155, vcc quad_perm:[1,0,3,2] row_mask:0xf bank_mask:0xf\n"
    "  v_mov_b32 v255, v255\n"
    "  v_cndmask_b32_dpp v68, v132, v140, vcc quad_perm:[1,0,3,2] row_mask:0xf bank_mask:0xf\n"
    "  v_mov_b32 v255, v255\n"
    "  v_cndmask_b32_dpp v69, v133, v141, vcc quad_perm:[1,0,3,2] row_mask:0xf bank_mask:0xf\n"
    "  v_mov_b32 v255, v255\n"
    "  v_cndmask_b32_dpp v70, v134, v142, vcc quad_perm:[1,0,3,2] row_mask:0xf bank_mask:0xf\n"
    "  v_mov_b32 v255, v255\n"
    "  v_cndmask_b32_dpp v71, v135, v143, vcc quad_perm:[1,0,3,2] row_mask:0xf bank_mask:0xf\n"
    "  v_mov_b32 v255, v255\n"
    "  v_cndmask_b32_dpp v76, v148, v156, vcc quad_perm:[1,0,3,2] row_mask:0xf bank_mask:0xf\n"
    "  v_mov_b32 v255, v255\n"
    "  v_cndmask_b32_dpp v77, v149, v157, vcc quad_perm:[1,0,3,2] row_mask:0xf bank_mask:0xf\n"
    "  v_mov_b32 v255, v255\n"
    "  v_cndmask_b32_dpp v78, v150, v158, vcc quad_perm:[1,0,3,2] row_mask:0xf bank_mask:0xf\n"
    "  v_mov_b32 v255, v255\n"
    "  v_cndmask_b32_dpp v79, v151, v159, vcc quad_perm:[1,0,3,2] row_mask:0xf bank_mask:0xf\n"
    "  s_mov_b64 vcc, s[62:63]\n"
    "  v_cndmask_b32_dpp v16, v56, v48, vcc quad_perm:[2,3,0,1] row_mask:0xf bank_mask:0xf\n"
    "  v_mov_b32 v255, v255\n"
    "  v_cndmask_b32_dpp v17, v57, v49, vcc quad_perm:[2,3,0,1] row_mask:0xf bank_mask:0xf\n"
    "  v_mov_b32 v255, v255\n"
    "  v_cndmask_b32_dpp v18, v58, v50, vcc quad_perm:[2,3,0,1] row_mask:0xf bank_mask:0xf\n"
    "  v_mov_b32 v255, v255\n"
    "  v_cndmask_b32_dpp v19, v59, v51, vcc quad_perm:[2,3,0,1] row_mask:0xf bank_mask:0xf\n"
    "  v_mov_b32 v255, v255\n"
    "  v_cndmask_b32_dpp v20, v60, v52, vcc quad_perm:[2,3,0,1] row_mask:0xf bank_mask:0xf\n"
    "  v_mov_b32 v255, v255\n"
    "  v_cndmask_b32_dpp v21, v61, v53, vcc quad_perm:[2,3,0,1] row_mask:0xf bank_mask:0xf\n"
    "  v_mov_b32 v255, v255\n"
    "  v_cndmask_b32_dpp v22, v62, v54, vcc quad_perm:[2,3,0,1] row_mask:0xf bank_mask:0xf\n"
    "  v_mov_b32 v255, v255\n"
    "  v_cndmask_b32_dpp v23, v63, v55, vcc quad_perm:[2,3,0,1] row_mask:0xf bank_mask:0xf\n"
    "  v_mov_b32 v255, v255\n"
    "  v_cndmask_b32_dpp v32, v72, v64, vcc quad_perm:[2,3,0,1] row_mask:0xf bank_mask:0xf\n"
    "  v_mov_b32 v255, v255\n"
    "  v_cndmask_b32_dpp v33, v73, v65, vcc quad_perm:[2,3,0,1] row_mask:0xf bank_mask:0xf\n"
    "  v_mov_b32 v255, v255\n"
    "  v_cndmask_b32_dpp v34, v74, v66, vcc quad_perm:[2,3,0,1] row_mask:0xf bank_mask:0xf\n"
    "  v_mov_b32 v255, v255\n"
    "  v_cndmask_b32_dpp v35, v75, v67, vcc quad_perm:[2,3,0,1] row_mask:0xf bank_mask:0xf\n"
    "  v_mov_b32 v255, v255\n"
    "  v_cndmask_b32_dpp v36, v76, v68, vcc quad_perm:[2,3,0,1] row_mask:0xf bank_mask:0xf\n"
    "  v_mov_b32 v255, v255\n"
    "  v_cndmask_b32_dpp v37, v77, v69, vcc quad_perm:[2,3,0,1] row_mask:0xf bank_mask:0xf\n"
    "  v_mov_b32 v255, v255\n"
    "  v_cndmask_b32_dpp v38, v78, v70, vcc quad_perm:[2,3,0,1] row_mask:0xf bank_mask:0xf\n"
    "  v_mov_b32 v255, v255\n"
    "  v_cndmask_b32_dpp v39, v79, v71, vcc quad_perm:[2,3,0,1] row_mask:0xf bank_mask:0xf\n"
    "  s_mov_b64 vcc, s[64:65]\n"
    "  v_cndmask_b32_dpp v24, v48, v56, vcc quad_perm:[2,3,0,1] row_mask:0xf bank_mask:0xf\n"
    "  v_mov_b32 v255, v255\n"
    "  v_cndmask_b32_dpp v25, v49, v57, vcc quad_perm:[2,3,0,1] row_mask:0xf bank_mask:0xf\n"
    "  v_mov_b32 v255, v255\n"
    "  v_cndmask_b32_dpp v26, v50, v58, vcc quad_perm:[2,3,0,1] row_mask:0xf bank_mask:0xf\n"
    "  v_mov_b32 v255, v255\n"
    "  v_cndmask_b32_dpp v27, v51, v59, vcc quad_perm:[2,3,0,1] row_mask:0xf bank_mask:0xf\n"
    "  v_mov_b32 v255, v255\n"
    "  v_cndmask_b32_dpp v28, v52, v60, vcc quad_perm:[2,3,0,1] row_mask:0xf bank_mask:0xf\n"
    "  v_mov_b32 v255, v255\n"
    "  v_cndmask_b32_dpp v29, v53, v61, vcc quad_perm:[2,3,0,1] row_mask:0xf bank_mask:0xf\n"
    "  v_mov_b32 v255, v255\n"
    "  v_cndmask_b32_dpp v30, v54, v62, vcc quad_perm:[2,3,0,1] row_mask:0xf bank_mask:0xf\n"
    "  v_mov_b32 v255, v255\n"
    "  v_cndmask_b32_dpp v31, v55, v63, vcc quad_perm:[2,3,0,1] row_mask:0xf bank_mask:0xf\n"
    "  v_mov_b32 v255, v255\n"
    "  v_cndmask_b32_dpp v40, v64, v72, vcc quad_perm:[2,3,0,1] row_mask:0xf bank_mask:0xf\n"
    "  v_mov_b32 v255, v255\n"
    "  v_cndmask_b32_dpp v41, v65, v73, vcc quad_perm:[2,3,0,1] row_mask:0xf bank_mask:0xf\n"
    "  v_mov_b32 v255, v255\n"
    "  v_cndmask_b32_dpp v42, v66, v74, vcc quad_perm:[2,3,0,1] row_mask:0xf bank_mask:0xf\n"
    "  v_mov_b32 v255, v255\n"
    "  v_cndmask_b32_dpp v43, v67, v75, vcc quad_perm:[2,3,0,1] row_mask:0xf bank_mask:0xf\n"
    "  v_mov_b32 v255, v255\n"
    "  v_cndmask_b32_dpp v44, v68, v76, vcc quad_perm:[2,3,0,1] row_mask:0xf bank_mask:0xf\n"
    "  v_mov_b32 v255, v255\n"
    "  v_cndmask_b32_dpp v45, v69, v77, vcc quad_perm:[2,3,0,1] row_mask:0xf bank_mask:0xf\n"
    "  v_mov_b32 v255, v255\n"
    "  v_cndmask_b32_dpp v46, v70, v78, vcc quad_perm:[2,3,0,1] row_mask:0xf bank_mask:0xf\n"
    "  v_mov_b32 v255, v255\n"
    "  v_cndmask_b32_dpp v47, v71, v79, vcc quad_perm:[2,3,0,1] row_mask:0xf bank_mask:0xf\n"
    "  s_barrier\n"
    "  s_branch .Lgqa_main\n"
    ".Lgqa_bpro:\n"
    "  s_setprio 3\n"
    "  s_cmp_lg_u32 s26, 7\n"
    "  s_cbranch_scc1 .Lgqa_nopf\n"
    "  s_getpc_b64 s[66:67]\n"
    ".Lgqa_pf:\n"
    "  v_lshlrev_b32 v11, 6, v14\n"
    "  v_add_u32 v15, 0, v11\n"
    "  v_min_u32 v15, .Lgqa_end-.Lgqa_pf-4, v15\n"
    "  global_load_dword v9, v15, s[66:67]\n"
    "  v_add_u32 v15, 4096, v11\n"
    "  v_min_u32 v15, .Lgqa_end-.Lgqa_pf-4, v15\n"
    "  global_load_dword v9, v15, s[66:67]\n"
    "  v_add_u32 v15, 8192, v11\n"
    "  v_min_u32 v15, .Lgqa_end-.Lgqa_pf-4, v15\n"
    "  global_load_dword v9, v15, s[66:67]\n"
    "  v_add_u32 v15, 12288, v11\n"
    "  v_min_u32 v15, .Lgqa_end-.Lgqa_pf-4, v15\n"
    "  global_load_dword v9, v15, s[66:67]\n"
    "  v_add_u32 v15, 16384, v11\n"
    "  v_min_u32 v15, .Lgqa_end-.Lgqa_pf-4, v15\n"
    "  global_load_dword v9, v15, s[66:67]\n"
    ".Lgqa_nopf:\n"
    "  s_lshl_b32 s57, s56, 11\n"
    "  s_lshl_b32 s66, s55, 7\n"
    "  s_or_b32 s57, s57, s66\n"
    "  s_lshl_b32 s66, s56, 2\n"
    "  v_lshrrev_b32 v15, 3, v14\n"
    "  v_lshlrev_b32 v12, 8, v15\n"
    "  v_or_b32 v12, s57, v12\n"
    "  v_lshrrev_b32 v11, 4, v14\n"
    "  v_or_b32 v11, s66, v11\n"
    "  v_xor_b32 v11, v11, v14\n"
    "  v_and_b32 v11, 7, v11\n"
    "  v_lshl_or_b32 v11, v11, 4, v12\n"
    "  v_and_b32 v15, 2, v15\n"
    "  v_xor_b32 v15, v15, v14\n"
    "  v_and_b32 v15, 7, v15\n"
    "  v_lshl_or_b32 v12, v15, 4, v12\n"
    "  s_lshl_b32 s30, s54, 10\n"
    "  s_add_u32 s30, s30, s38\n"
    "  s_bfe_u32 s36, s37, 0x20003\n"
    "  s_mul_i32 s36, s36, 20480\n"
    "  s_waitcnt lgkmcnt(0)\n"
    "  s_add_u32 s8, s42, s52\n"
    "  s_addc_u32 s9, s43, 0\n"
    "  s_and_b32 s9, s9, 0xffff\n"
    "  s_mov_b32 s10, 0x14000\n"
    "  s_mov_b32 s11, 0x20000\n"
    "  s_add_u32 m0, s30, 0\n"
    "  s_movk_i32 s66, 0\n"
    "  buffer_load_dwordx4 v11, s[8:11], s66 offen nt lds\n"
    "  s_add_u32 m0, s30, 4096\n"
    "  s_movk_i32 s66, 4096\n"
    "  buffer_load_dwordx4 v11, s[8:11], s66 offen nt lds\n"
    "  s_add_u32 m0, s30, 8192\n"
    "  s_movk_i32 s66, 8192\n"
    "  buffer_load_dwordx4 v11, s[8:11], s66 offen nt lds\n"
    "  s_add_u32 m0, s30, 12288\n"
    "  s_movk_i32 s66, 12288\n"
    "  buffer_load_dwordx4 v11, s[8:11], s66 offen nt lds\n"
    "  s_add_u32 s4, s40, s53\n"
    "  s_addc_u32 s5, s41, 0\n"
    "  s_and_b32 s5, s5, 0xffff\n"
    "  s_mov_b32 s6, 0x14000\n"
    "  s_mov_b32 s7, 0x20000\n"
    "  s_add_u32 s12, s44, s52\n"
    "  s_addc_u32 s13, s45, 0\n"
    "  s_and_b32 s13, s13, 0xffff\n"
    "  s_mov_b32 s14, 0x14000\n"
    "  s_mov_b32 s15, 0x20000\n"
    "  v_lshlrev_b32 v13, 2, v14\n"
    "  s_add_u32 s16, s46, s53\n"
    "  s_addc_u32 s17, s47, 0\n"
    "  s_and_b32 s17, s17, 0xffff\n"
    "  s_mov_b32 s18, 0x14000\n"
    "  s_mov_b32 s19, 0x20000\n"
    "  s_lshl_b32 s57, s37, 7\n"
    "  s_add_u32 s24, s48, s57\n"
    "  s_addc_u32 s25, s49, 0\n"
    "  s_mov_b32 s20, s24\n"
    "  s_and_b32 s21, s25, 0xffff\n"
    "  s_mov_b32 s22, 64\n"
    "  s_mov_b32 s23, 0x20000\n"
    "  v_lshlrev_b32 v1, 8, v14\n"
    "  v_and_b32 v1, 0x1800, v1\n"
    "  v_lshlrev_b32 v15, 7, v14\n"
    "  v_and_b32 v15, 0x380, v15\n"
    "  v_or_b32 v1, v1, v15\n"
    "  v_lshrrev_b32 v15, 3, v14\n"
    "  v_lshrrev_b32 v2, 1, v14\n"
    "  v_xor_b32 v15, v15, v2\n"
    "  v_and_b32 v15, 4, v15\n"
    "  v_and_b32 v2, 3, v2\n"
    "  v_or_b32 v15, v15, v2\n"
    "  v_lshl_or_b32 v1, v15, 4, v1\n"
    "  v_add_u32 v1, s38, v1\n"
    "  v_xor_b32 v2, 16, v1\n"
    "  v_xor_b32 v3, 32, v1\n"
    "  v_xor_b32 v4, 48, v1\n"
    "  v_lshrrev_b32 v15, 5, v14\n"
    "  v_lshlrev_b32 v5, 9, v15\n"
    "  v_lshlrev_b32 v15, 5, v14\n"
    "  v_and_b32 v15, 0x180, v15\n"
    "  v_or_b32 v5, v5, v15\n"
    "  v_lshlrev_b32 v15, 6, v14\n"
    "  v_and_b32 v15, 64, v15\n"
    "  v_or_b32 v5, v5, v15\n"
    "  v_and_b32 v15, 16, v14\n"
    "  v_or_b32 v5, v5, v15\n"
    "  v_lshlrev_b32 v15, 2, v14\n"
    "  v_and_b32 v15, 40, v15\n"
    "  v_or_b32 v5, v5, v15\n"
    "  s_add_u32 s57, s38, 0x14000\n"
    "  v_add_u32 v5, s57, v5\n"
    "  v_xor_b32 v6, 32, v5\n"
    "  v_add_u32 v7, 0x400, v5\n"
    "  v_add_u32 v8, 0x400, v6\n"
    "  s_lshl_b32 s28, s56, 13\n"
    "  s_add_u32 s28, s28, 0x10000\n"
    "  s_lshl_b32 s57, s55, 7\n"
    "  s_add_u32 s29, s28, s57\n"
    "  s_lshl_b32 s42, s54, 2\n"
    "  s_lshl_b32 s43, s26, 14\n"
    "  s_add_u32 s43, s43, s38\n"
    "  s_cmp_eq_u32 s55, 0\n"
    "  s_cselect_b32 s31, 0xa000, 0\n"
    "  s_lshl_b32 s57, s55, 10\n"
    "  s_sub_u32 s40, s57, s31\n"
    "  s_add_u32 s57, s57, s31\n"
    "  s_sub_u32 s41, 0, s57\n"
    "  s_lshl_b32 s57, s55, 15\n"
    "  s_sub_u32 s44, 0x4000, s57\n"
    "  s_xor_b32 s45, s42, 4\n"
    "  s_add_u32 s45, s45, 16\n"
    "  s_mov_b32 s58, 0x55555555\n"
    "  s_mov_b32 s59, 0x55555555\n"
    "  s_mov_b32 s60, 0xaaaaaaaa\n"
    "  s_mov_b32 s61, 0xaaaaaaaa\n"
    "  s_mov_b32 s62, 0x33333333\n"
    "  s_mov_b32 s63, 0x33333333\n"
    "  s_mov_b32 s64, 0xcccccccc\n"
    "  s_mov_b32 s65, 0xcccccccc\n"
    "  v_mov_b32 v244, 0xff7fffff\n"
    "  v_mov_b32 v245, 0\n"
    "  v_mov_b32 v246, 0\n"
    "  v_mov_b32 v248, 0x3e0293ee\n"
    "  s_add_u32 m0, s30, 16384\n"
    "  s_movk_i32 s66, 16384\n"
    "  buffer_load_dwordx4 v11, s[8:11], s66 offen nt lds\n"
    "  s_add_u32 m0, s30, 20480\n"
    "  s_movk_i32 s66, 20480\n"
    "  buffer_load_dwordx4 v11, s[8:11], s66 offen nt lds\n"
    "  s_add_u32 m0, s30, 24576\n"
    "  s_movk_i32 s66, 24576\n"
    "  buffer_load_dwordx4 v11, s[8:11], s66 offen nt lds\n"
    "  s_add_u32 m0, s30, 28672\n"
    "  s_movk_i32 s66, 28672\n"
    "  buffer_load_dwordx4 v11, s[8:11], s66 offen nt lds\n"
    "  s_add_u32 m0, s30, 32768\n"
    "  s_mov_b32 s66, 32768\n"
    "  buffer_load_dwordx4 v11, s[8:11], s66 offen nt lds\n"
    "  s_add_u32 m0, s30, 36864\n"
    "  s_mov_b32 s66, 36864\n"
    "  buffer_load_dwordx4 v11, s[8:11], s66 offen nt lds\n"
    "  s_waitcnt vmcnt(6)\n"
    "  s_barrier\n"
    "  s_add_u32 m0, s30, 40960\n"
    "  s_mov_b32 s66, 40960\n"
    "  buffer_load_dwordx4 v11, s[8:11], s66 offen nt lds\n"
    "  s_add_u32 m0, s30, 45056\n"
    "  s_mov_b32 s66, 45056\n"
    "  buffer_load_dwordx4 v11, s[8:11], s66 offen nt lds\n"
    "  s_add_u32 m0, s30, 49152\n"
    "  s_mov_b32 s66, 49152\n"
    "  buffer_load_dwordx4 v11, s[8:11], s66 offen nt lds\n"
    "  s_add_u32 m0, s30, 53248\n"
    "  s_mov_b32 s66, 53248\n"
    "  buffer_load_dwordx4 v11, s[8:11], s66 offen nt lds\n"
    "  s_add_u32 m0, s30, 57344\n"
    "  s_mov_b32 s66, 57344\n"
    "  buffer_load_dwordx4 v11, s[8:11], s66 offen nt lds\n"
    "  s_add_u32 m0, s30, 61440\n"
    "  s_mov_b32 s66, 61440\n"
    "  buffer_load_dwordx4 v11, s[8:11], s66 offen nt lds\n"
    "  s_waitcnt vmcnt(6)\n"
    "  s_barrier\n"
    "  s_add_u32 m0, s30, 65536\n"
    "  s_mov_b32 s66, 65536\n"
    "  buffer_load_dwordx4 v11, s[8:11], s66 offen nt lds\n"
    "  s_add_u32 m0, s30, 69632\n"
    "  s_mov_b32 s66, 69632\n"
    "  buffer_load_dwordx4 v11, s[8:11], s66 offen nt lds\n"
    "  s_add_u32 m0, s30, 73728\n"
    "  s_mov_b32 s66, 73728\n"
    "  buffer_load_dwordx4 v11, s[8:11], s66 offen nt lds\n"
    "  s_add_u32 m0, s30, 77824\n"
    "  s_mov_b32 s66, 77824\n"
    "  buffer_load_dwordx4 v11, s[8:11], s66 offen nt lds\n"
    "  buffer_load_dwordx4 v[128:131], v10, s[4:7], s27 offen offset:0 nt\n"
    "  buffer_load_dwordx4 v[132:135], v10, s[4:7], s27 offen offset:128 nt\n"
    "  buffer_load_dwordx4 v[136:139], v10, s[4:7], s27 offen offset:256 nt\n"
    "  buffer_load_dwordx4 v[140:143], v10, s[4:7], s27 offen offset:384 nt\n"
    "  buffer_load_dwordx4 v[144:147], v10, s[4:7], s27 offen offset:512 nt\n"
    "  buffer_load_dwordx4 v[148:151], v10, s[4:7], s27 offen offset:640 nt\n"
    "  buffer_load_dwordx4 v[152:155], v10, s[4:7], s27 offen offset:768 nt\n"
    "  buffer_load_dwordx4 v[156:159], v10, s[4:7], s27 offen offset:896 nt\n"
    "  s_waitcnt vmcnt(8)\n"
    "  s_barrier\n"
    "  s_add_u32 m0, s30, 81920\n"
    "  s_movk_i32 s66, 0\n"
    "  buffer_load_dwordx4 v12, s[12:15], s66 offen nt lds\n"
    "  s_add_u32 m0, s30, 86016\n"
    "  s_movk_i32 s66, 4096\n"
    "  buffer_load_dwordx4 v12, s[12:15], s66 offen nt lds\n"
    "  s_add_u32 m0, s30, 90112\n"
    "  s_movk_i32 s66, 8192\n"
    "  buffer_load_dwordx4 v12, s[12:15], s66 offen nt lds\n"
    "  s_add_u32 m0, s30, 94208\n"
    "  s_movk_i32 s66, 12288\n"
    "  buffer_load_dwordx4 v12, s[12:15], s66 offen nt lds\n"
    "  s_add_u32 m0, s30, 98304\n"
    "  s_movk_i32 s66, 16384\n"
    "  buffer_load_dwordx4 v12, s[12:15], s66 offen nt lds\n"
    "  s_add_u32 m0, s30, 102400\n"
    "  s_movk_i32 s66, 20480\n"
    "  buffer_load_dwordx4 v12, s[12:15], s66 offen nt lds\n"
    "  s_add_u32 m0, s30, 106496\n"
    "  s_movk_i32 s66, 24576\n"
    "  buffer_load_dwordx4 v12, s[12:15], s66 offen nt lds\n"
    "  s_add_u32 m0, s30, 110592\n"
    "  s_movk_i32 s66, 28672\n"
    "  buffer_load_dwordx4 v12, s[12:15], s66 offen nt lds\n"
    "  s_add_u32 m0, s30, 114688\n"
    "  s_mov_b32 s66, 32768\n"
    "  buffer_load_dwordx4 v12, s[12:15], s66 offen nt lds\n"
    "  s_add_u32 m0, s30, 118784\n"
    "  s_mov_b32 s66, 36864\n"
    "  buffer_load_dwordx4 v12, s[12:15], s66 offen nt lds\n"
    "  s_waitcnt vmcnt(10)\n"
    "  s_mov_b64 vcc, s[58:59]\n"
    "  v_cndmask_b32_dpp v48, v136, v128, vcc quad_perm:[1,0,3,2] row_mask:0xf bank_mask:0xf\n"
    "  v_mov_b32 v255, v255\n"
    "  v_cndmask_b32_dpp v49, v137, v129, vcc quad_perm:[1,0,3,2] row_mask:0xf bank_mask:0xf\n"
    "  v_mov_b32 v255, v255\n"
    "  v_cndmask_b32_dpp v50, v138, v130, vcc quad_perm:[1,0,3,2] row_mask:0xf bank_mask:0xf\n"
    "  v_mov_b32 v255, v255\n"
    "  v_cndmask_b32_dpp v51, v139, v131, vcc quad_perm:[1,0,3,2] row_mask:0xf bank_mask:0xf\n"
    "  v_mov_b32 v255, v255\n"
    "  v_cndmask_b32_dpp v56, v152, v144, vcc quad_perm:[1,0,3,2] row_mask:0xf bank_mask:0xf\n"
    "  v_mov_b32 v255, v255\n"
    "  v_cndmask_b32_dpp v57, v153, v145, vcc quad_perm:[1,0,3,2] row_mask:0xf bank_mask:0xf\n"
    "  v_mov_b32 v255, v255\n"
    "  v_cndmask_b32_dpp v58, v154, v146, vcc quad_perm:[1,0,3,2] row_mask:0xf bank_mask:0xf\n"
    "  v_mov_b32 v255, v255\n"
    "  v_cndmask_b32_dpp v59, v155, v147, vcc quad_perm:[1,0,3,2] row_mask:0xf bank_mask:0xf\n"
    "  v_mov_b32 v255, v255\n"
    "  v_cndmask_b32_dpp v64, v140, v132, vcc quad_perm:[1,0,3,2] row_mask:0xf bank_mask:0xf\n"
    "  v_mov_b32 v255, v255\n"
    "  v_cndmask_b32_dpp v65, v141, v133, vcc quad_perm:[1,0,3,2] row_mask:0xf bank_mask:0xf\n"
    "  v_mov_b32 v255, v255\n"
    "  v_cndmask_b32_dpp v66, v142, v134, vcc quad_perm:[1,0,3,2] row_mask:0xf bank_mask:0xf\n"
    "  v_mov_b32 v255, v255\n"
    "  v_cndmask_b32_dpp v67, v143, v135, vcc quad_perm:[1,0,3,2] row_mask:0xf bank_mask:0xf\n"
    "  v_mov_b32 v255, v255\n"
    "  v_cndmask_b32_dpp v72, v156, v148, vcc quad_perm:[1,0,3,2] row_mask:0xf bank_mask:0xf\n"
    "  v_mov_b32 v255, v255\n"
    "  v_cndmask_b32_dpp v73, v157, v149, vcc quad_perm:[1,0,3,2] row_mask:0xf bank_mask:0xf\n"
    "  v_mov_b32 v255, v255\n"
    "  v_cndmask_b32_dpp v74, v158, v150, vcc quad_perm:[1,0,3,2] row_mask:0xf bank_mask:0xf\n"
    "  v_mov_b32 v255, v255\n"
    "  v_cndmask_b32_dpp v75, v159, v151, vcc quad_perm:[1,0,3,2] row_mask:0xf bank_mask:0xf\n"
    "  s_mov_b64 vcc, s[60:61]\n"
    "  v_cndmask_b32_dpp v52, v128, v136, vcc quad_perm:[1,0,3,2] row_mask:0xf bank_mask:0xf\n"
    "  v_mov_b32 v255, v255\n"
    "  v_cndmask_b32_dpp v53, v129, v137, vcc quad_perm:[1,0,3,2] row_mask:0xf bank_mask:0xf\n"
    "  v_mov_b32 v255, v255\n"
    "  v_cndmask_b32_dpp v54, v130, v138, vcc quad_perm:[1,0,3,2] row_mask:0xf bank_mask:0xf\n"
    "  v_mov_b32 v255, v255\n"
    "  v_cndmask_b32_dpp v55, v131, v139, vcc quad_perm:[1,0,3,2] row_mask:0xf bank_mask:0xf\n"
    "  v_mov_b32 v255, v255\n"
    "  v_cndmask_b32_dpp v60, v144, v152, vcc quad_perm:[1,0,3,2] row_mask:0xf bank_mask:0xf\n"
    "  v_mov_b32 v255, v255\n"
    "  v_cndmask_b32_dpp v61, v145, v153, vcc quad_perm:[1,0,3,2] row_mask:0xf bank_mask:0xf\n"
    "  v_mov_b32 v255, v255\n"
    "  v_cndmask_b32_dpp v62, v146, v154, vcc quad_perm:[1,0,3,2] row_mask:0xf bank_mask:0xf\n"
    "  v_mov_b32 v255, v255\n"
    "  v_cndmask_b32_dpp v63, v147, v155, vcc quad_perm:[1,0,3,2] row_mask:0xf bank_mask:0xf\n"
    "  v_mov_b32 v255, v255\n"
    "  v_cndmask_b32_dpp v68, v132, v140, vcc quad_perm:[1,0,3,2] row_mask:0xf bank_mask:0xf\n"
    "  v_mov_b32 v255, v255\n"
    "  v_cndmask_b32_dpp v69, v133, v141, vcc quad_perm:[1,0,3,2] row_mask:0xf bank_mask:0xf\n"
    "  v_mov_b32 v255, v255\n"
    "  v_cndmask_b32_dpp v70, v134, v142, vcc quad_perm:[1,0,3,2] row_mask:0xf bank_mask:0xf\n"
    "  v_mov_b32 v255, v255\n"
    "  v_cndmask_b32_dpp v71, v135, v143, vcc quad_perm:[1,0,3,2] row_mask:0xf bank_mask:0xf\n"
    "  v_mov_b32 v255, v255\n"
    "  v_cndmask_b32_dpp v76, v148, v156, vcc quad_perm:[1,0,3,2] row_mask:0xf bank_mask:0xf\n"
    "  v_mov_b32 v255, v255\n"
    "  v_cndmask_b32_dpp v77, v149, v157, vcc quad_perm:[1,0,3,2] row_mask:0xf bank_mask:0xf\n"
    "  v_mov_b32 v255, v255\n"
    "  v_cndmask_b32_dpp v78, v150, v158, vcc quad_perm:[1,0,3,2] row_mask:0xf bank_mask:0xf\n"
    "  v_mov_b32 v255, v255\n"
    "  v_cndmask_b32_dpp v79, v151, v159, vcc quad_perm:[1,0,3,2] row_mask:0xf bank_mask:0xf\n"
    "  s_mov_b64 vcc, s[62:63]\n"
    "  v_cndmask_b32_dpp v16, v56, v48, vcc quad_perm:[2,3,0,1] row_mask:0xf bank_mask:0xf\n"
    "  v_mov_b32 v255, v255\n"
    "  v_cndmask_b32_dpp v17, v57, v49, vcc quad_perm:[2,3,0,1] row_mask:0xf bank_mask:0xf\n"
    "  v_mov_b32 v255, v255\n"
    "  v_cndmask_b32_dpp v18, v58, v50, vcc quad_perm:[2,3,0,1] row_mask:0xf bank_mask:0xf\n"
    "  v_mov_b32 v255, v255\n"
    "  v_cndmask_b32_dpp v19, v59, v51, vcc quad_perm:[2,3,0,1] row_mask:0xf bank_mask:0xf\n"
    "  v_mov_b32 v255, v255\n"
    "  v_cndmask_b32_dpp v20, v60, v52, vcc quad_perm:[2,3,0,1] row_mask:0xf bank_mask:0xf\n"
    "  v_mov_b32 v255, v255\n"
    "  v_cndmask_b32_dpp v21, v61, v53, vcc quad_perm:[2,3,0,1] row_mask:0xf bank_mask:0xf\n"
    "  v_mov_b32 v255, v255\n"
    "  v_cndmask_b32_dpp v22, v62, v54, vcc quad_perm:[2,3,0,1] row_mask:0xf bank_mask:0xf\n"
    "  v_mov_b32 v255, v255\n"
    "  v_cndmask_b32_dpp v23, v63, v55, vcc quad_perm:[2,3,0,1] row_mask:0xf bank_mask:0xf\n"
    "  v_mov_b32 v255, v255\n"
    "  v_cndmask_b32_dpp v32, v72, v64, vcc quad_perm:[2,3,0,1] row_mask:0xf bank_mask:0xf\n"
    "  v_mov_b32 v255, v255\n"
    "  v_cndmask_b32_dpp v33, v73, v65, vcc quad_perm:[2,3,0,1] row_mask:0xf bank_mask:0xf\n"
    "  v_mov_b32 v255, v255\n"
    "  v_cndmask_b32_dpp v34, v74, v66, vcc quad_perm:[2,3,0,1] row_mask:0xf bank_mask:0xf\n"
    "  v_mov_b32 v255, v255\n"
    "  v_cndmask_b32_dpp v35, v75, v67, vcc quad_perm:[2,3,0,1] row_mask:0xf bank_mask:0xf\n"
    "  v_mov_b32 v255, v255\n"
    "  v_cndmask_b32_dpp v36, v76, v68, vcc quad_perm:[2,3,0,1] row_mask:0xf bank_mask:0xf\n"
    "  v_mov_b32 v255, v255\n"
    "  v_cndmask_b32_dpp v37, v77, v69, vcc quad_perm:[2,3,0,1] row_mask:0xf bank_mask:0xf\n"
    "  v_mov_b32 v255, v255\n"
    "  v_cndmask_b32_dpp v38, v78, v70, vcc quad_perm:[2,3,0,1] row_mask:0xf bank_mask:0xf\n"
    "  v_mov_b32 v255, v255\n"
    "  v_cndmask_b32_dpp v39, v79, v71, vcc quad_perm:[2,3,0,1] row_mask:0xf bank_mask:0xf\n"
    "  s_mov_b64 vcc, s[64:65]\n"
    "  v_cndmask_b32_dpp v24, v48, v56, vcc quad_perm:[2,3,0,1] row_mask:0xf bank_mask:0xf\n"
    "  v_mov_b32 v255, v255\n"
    "  v_cndmask_b32_dpp v25, v49, v57, vcc quad_perm:[2,3,0,1] row_mask:0xf bank_mask:0xf\n"
    "  v_mov_b32 v255, v255\n"
    "  v_cndmask_b32_dpp v26, v50, v58, vcc quad_perm:[2,3,0,1] row_mask:0xf bank_mask:0xf\n"
    "  v_mov_b32 v255, v255\n"
    "  v_cndmask_b32_dpp v27, v51, v59, vcc quad_perm:[2,3,0,1] row_mask:0xf bank_mask:0xf\n"
    "  v_mov_b32 v255, v255\n"
    "  v_cndmask_b32_dpp v28, v52, v60, vcc quad_perm:[2,3,0,1] row_mask:0xf bank_mask:0xf\n"
    "  v_mov_b32 v255, v255\n"
    "  v_cndmask_b32_dpp v29, v53, v61, vcc quad_perm:[2,3,0,1] row_mask:0xf bank_mask:0xf\n"
    "  v_mov_b32 v255, v255\n"
    "  v_cndmask_b32_dpp v30, v54, v62, vcc quad_perm:[2,3,0,1] row_mask:0xf bank_mask:0xf\n"
    "  v_mov_b32 v255, v255\n"
    "  v_cndmask_b32_dpp v31, v55, v63, vcc quad_perm:[2,3,0,1] row_mask:0xf bank_mask:0xf\n"
    "  v_mov_b32 v255, v255\n"
    "  v_cndmask_b32_dpp v40, v64, v72, vcc quad_perm:[2,3,0,1] row_mask:0xf bank_mask:0xf\n"
    "  v_mov_b32 v255, v255\n"
    "  v_cndmask_b32_dpp v41, v65, v73, vcc quad_perm:[2,3,0,1] row_mask:0xf bank_mask:0xf\n"
    "  v_mov_b32 v255, v255\n"
    "  v_cndmask_b32_dpp v42, v66, v74, vcc quad_perm:[2,3,0,1] row_mask:0xf bank_mask:0xf\n"
    "  v_mov_b32 v255, v255\n"
    "  v_cndmask_b32_dpp v43, v67, v75, vcc quad_perm:[2,3,0,1] row_mask:0xf bank_mask:0xf\n"
    "  v_mov_b32 v255, v255\n"
    "  v_cndmask_b32_dpp v44, v68, v76, vcc quad_perm:[2,3,0,1] row_mask:0xf bank_mask:0xf\n"
    "  v_mov_b32 v255, v255\n"
    "  v_cndmask_b32_dpp v45, v69, v77, vcc quad_perm:[2,3,0,1] row_mask:0xf bank_mask:0xf\n"
    "  v_mov_b32 v255, v255\n"
    "  v_cndmask_b32_dpp v46, v70, v78, vcc quad_perm:[2,3,0,1] row_mask:0xf bank_mask:0xf\n"
    "  v_mov_b32 v255, v255\n"
    "  v_cndmask_b32_dpp v47, v71, v79, vcc quad_perm:[2,3,0,1] row_mask:0xf bank_mask:0xf\n"
    ".Lgqa_main:\n"
    "  s_nop 1\n"
    "  ds_read_b128 v[224:227], v1 offset:0\n"
    "  ds_read_b128 v[228:231], v2 offset:0\n"
    "  ds_read_b128 v[232:235], v3 offset:0\n"
    "  ds_read_b128 v[236:239], v4 offset:0\n"
    "  s_waitcnt lgkmcnt(2)\n"
    "  v_mfma_f32_32x32x16_bf16 v[48:63], v[224:227], v[16:19], 0\n"
    "  ds_read_b128 v[240:243], v1 offset:1024\n"
    "  v_mfma_f32_32x32x16_bf16 v[48:63], v[228:231], v[20:23], v[48:63]\n"
    "  ds_read_b128 v[224:227], v2 offset:1024\n"
    "  s_waitcnt lgkmcnt(2)\n"
    "  v_mfma_f32_32x32x16_bf16 v[48:63], v[232:235], v[24:27], v[48:63]\n"
    "  ds_read_b128 v[228:231], v3 offset:1024\n"
    "  v_mfma_f32_32x32x16_bf16 v[48:63], v[236:239], v[28:31], v[48:63]\n"
    "  ds_read_b128 v[232:235], v4 offset:1024\n"
    "  s_waitcnt lgkmcnt(2)\n"
    "  v_mfma_f32_32x32x16_bf16 v[48:63], v[240:243], v[32:35], v[48:63]\n"
    "  ds_read_b128 v[236:239], v1 offset:8192\n"
    "  v_mfma_f32_32x32x16_bf16 v[48:63], v[224:227], v[36:39], v[48:63]\n"
    "  ds_read_b128 v[240:243], v2 offset:8192\n"
    "  s_cmp_lt_u32 s26, 4\n"
    "  s_cbranch_scc1 .Lgqa_bvi\n"
    "  s_add_u32 m0, s30, 122880\n"
    "  s_mov_b32 s66, 40960\n"
    "  buffer_load_dwordx4 v12, s[12:15], s66 offen nt lds\n"
    "  s_add_u32 m0, s30, 126976\n"
    "  s_mov_b32 s66, 45056\n"
    "  buffer_load_dwordx4 v12, s[12:15], s66 offen nt lds\n"
    "  s_add_u32 m0, s30, 131072\n"
    "  s_mov_b32 s66, 49152\n"
    "  buffer_load_dwordx4 v12, s[12:15], s66 offen nt lds\n"
    "  s_add_u32 m0, s30, 135168\n"
    "  s_mov_b32 s66, 53248\n"
    "  buffer_load_dwordx4 v12, s[12:15], s66 offen nt lds\n"
    "  s_add_u32 m0, s30, 139264\n"
    "  s_mov_b32 s66, 57344\n"
    "  buffer_load_dwordx4 v12, s[12:15], s66 offen nt lds\n"
    "  s_add_u32 m0, s30, 143360\n"
    "  s_mov_b32 s66, 61440\n"
    "  buffer_load_dwordx4 v12, s[12:15], s66 offen nt lds\n"
    "  s_add_u32 m0, s30, 147456\n"
    "  s_mov_b32 s66, 65536\n"
    "  buffer_load_dwordx4 v12, s[12:15], s66 offen nt lds\n"
    "  s_add_u32 m0, s30, 151552\n"
    "  s_mov_b32 s66, 69632\n"
    "  buffer_load_dwordx4 v12, s[12:15], s66 offen nt lds\n"
    "  s_add_u32 m0, s30, 155648\n"
    "  s_mov_b32 s66, 73728\n"
    "  buffer_load_dwordx4 v12, s[12:15], s66 offen nt lds\n"
    "  s_add_u32 m0, s30, 159744\n"
    "  s_mov_b32 s66, 77824\n"
    "  buffer_load_dwordx4 v12, s[12:15], s66 offen nt lds\n"
    ".Lgqa_bvi:\n"
    "  s_waitcnt lgkmcnt(2)\n"
    "  v_mfma_f32_32x32x16_bf16 v[48:63], v[228:231], v[40:43], v[48:63]\n"
    "  ds_read_b128 v[224:227], v3 offset:8192\n"
    "  v_mfma_f32_32x32x16_bf16 v[48:63], v[232:235], v[44:47], v[48:63]\n"
    "  ds_read_b128 v[228:231], v4 offset:8192\n"
    "  s_waitcnt lgkmcnt(2)\n"
    "  v_mfma_f32_32x32x16_bf16 v[64:79], v[236:239], v[16:19], 0\n"
    "  ds_read_b128 v[232:235], v1 offset:9216\n"
    "  v_mfma_f32_32x32x16_bf16 v[64:79], v[240:243], v[20:23], v[64:79]\n"
    "  ds_read_b128 v[236:239], v2 offset:9216\n"
    "  s_cmp_lt_u32 s26, 4\n"
    "  s_cbranch_scc1 .Lgqa_bv10\n"
    "  s_waitcnt vmcnt(10)\n"
    "  v_mov_b32 v250, 1\n"
    "  v_lshlrev_b32 v251, 10, v13\n"
    "  buffer_store_dword v250, v251, s[20:23], s42 offen offset:0\n"
    ".Lgqa_bv10:\n"
    "  s_waitcnt lgkmcnt(2)\n"
    "  v_mfma_f32_32x32x16_bf16 v[64:79], v[224:227], v[24:27], v[64:79]\n"
    "  ds_read_b128 v[240:243], v3 offset:9216\n"
    "  v_mfma_f32_32x32x16_bf16 v[64:79], v[228:231], v[28:31], v[64:79]\n"
    "  ds_read_b128 v[224:227], v4 offset:9216\n"
    "  v_max3_f32 v244, v244, v48, v49\n"
    "  v_max3_f32 v244, v244, v50, v51\n"
    "  s_cmp_gt_u32 s26, 3\n"
    "  s_cbranch_scc1 .Lgqa_sk16\n"
    "  s_barrier\n"
    ".Lgqa_sk16:\n"
    "  s_waitcnt lgkmcnt(2)\n"
    "  v_mfma_f32_32x32x16_bf16 v[64:79], v[232:235], v[32:35], v[64:79]\n"
    "  ds_read_b128 v[228:231], v1 offset:16384\n"
    "  v_max3_f32 v244, v244, v52, v53\n"
    "  v_max3_f32 v244, v244, v54, v55\n"
    "  v_mfma_f32_32x32x16_bf16 v[64:79], v[236:239], v[36:39], v[64:79]\n"
    "  ds_read_b128 v[232:235], v2 offset:16384\n"
    "  v_max3_f32 v244, v244, v56, v57\n"
    "  s_waitcnt lgkmcnt(2)\n"
    "  v_mfma_f32_32x32x16_bf16 v[64:79], v[240:243], v[40:43], v[64:79]\n"
    "  ds_read_b128 v[236:239], v3 offset:16384\n"
    "  v_max3_f32 v244, v244, v58, v59\n"
    "  v_max3_f32 v244, v244, v60, v61\n"
    "  v_mfma_f32_32x32x16_bf16 v[64:79], v[224:227], v[44:47], v[64:79]\n"
    "  ds_read_b128 v[240:243], v4 offset:16384\n"
    "  v_max3_f32 v244, v244, v62, v63\n"
    "  s_waitcnt lgkmcnt(2)\n"
    "  v_mfma_f32_32x32x16_bf16 v[80:95], v[228:231], v[16:19], 0\n"
    "  ds_read_b128 v[224:227], v1 offset:17408\n"
    "  v_mfma_f32_32x32x16_bf16 v[80:95], v[232:235], v[20:23], v[80:95]\n"
    "  ds_read_b128 v[228:231], v2 offset:17408\n"
    "  s_waitcnt lgkmcnt(2)\n"
    "  v_mfma_f32_32x32x16_bf16 v[80:95], v[236:239], v[24:27], v[80:95]\n"
    "  ds_read_b128 v[232:235], v3 offset:17408\n"
    "  v_mfma_f32_32x32x16_bf16 v[80:95], v[240:243], v[28:31], v[80:95]\n"
    "  ds_read_b128 v[236:239], v4 offset:17408\n"
    "  v_max3_f32 v244, v244, v64, v65\n"
    "  v_max3_f32 v244, v244, v66, v67\n"
    "  s_waitcnt lgkmcnt(2)\n"
    "  v_mfma_f32_32x32x16_bf16 v[80:95], v[224:227], v[32:35], v[80:95]\n"
    "  ds_read_b128 v[240:243], v1 offset:24576\n"
    "  v_max3_f32 v244, v244, v68, v69\n"
    "  v_max3_f32 v244, v244, v70, v71\n"
    "  v_mfma_f32_32x32x16_bf16 v[80:95], v[228:231], v[36:39], v[80:95]\n"
    "  ds_read_b128 v[224:227], v2 offset:24576\n"
    "  v_max3_f32 v244, v244, v72, v73\n"
    "  s_waitcnt lgkmcnt(2)\n"
    "  v_mfma_f32_32x32x16_bf16 v[80:95], v[232:235], v[40:43], v[80:95]\n"
    "  ds_read_b128 v[228:231], v3 offset:24576\n"
    "  v_max3_f32 v244, v244, v74, v75\n"
    "  v_max3_f32 v244, v244, v76, v77\n"
    "  v_mfma_f32_32x32x16_bf16 v[80:95], v[236:239], v[44:47], v[80:95]\n"
    "  ds_read_b128 v[232:235], v4 offset:24576\n"
    "  v_max3_f32 v244, v244, v78, v79\n"
    "  s_cmp_lt_u32 s26, 4\n"
    "  s_cbranch_scc1 .Lgqa_bv24\n"
    "  s_waitcnt vmcnt(0)\n"
    "  v_mov_b32 v250, 2\n"
    "  v_lshlrev_b32 v251, 10, v13\n"
    "  buffer_store_dword v250, v251, s[20:23], s42 offen offset:0\n"
    "  s_setprio 2\n"
    ".Lgqa_bv24:\n"
    "  s_waitcnt lgkmcnt(2)\n"
    "  v_mfma_f32_32x32x16_bf16 v[96:111], v[240:243], v[16:19], 0\n"
    "  ds_read_b128 v[236:239], v1 offset:25600\n"
    "  v_mfma_f32_32x32x16_bf16 v[96:111], v[224:227], v[20:23], v[96:111]\n"
    "  ds_read_b128 v[240:243], v2 offset:25600\n"
    "  s_waitcnt lgkmcnt(2)\n"
    "  v_mfma_f32_32x32x16_bf16 v[96:111], v[228:231], v[24:27], v[96:111]\n"
    "  ds_read_b128 v[224:227], v3 offset:25600\n"
    "  v_mfma_f32_32x32x16_bf16 v[96:111], v[232:235], v[28:31], v[96:111]\n"
    "  ds_read_b128 v[228:231], v4 offset:25600\n"
    "  v_max3_f32 v244, v244, v80, v81\n"
    "  v_max3_f32 v244, v244, v82, v83\n"
    "  s_waitcnt lgkmcnt(2)\n"
    "  v_mfma_f32_32x32x16_bf16 v[96:111], v[236:239], v[32:35], v[96:111]\n"
    "  ds_read_b128 v[232:235], v1 offset:32768\n"
    "  v_max3_f32 v244, v244, v84, v85\n"
    "  v_max3_f32 v244, v244, v86, v87\n"
    "  v_mfma_f32_32x32x16_bf16 v[96:111], v[240:243], v[36:39], v[96:111]\n"
    "  ds_read_b128 v[236:239], v2 offset:32768\n"
    "  v_max3_f32 v244, v244, v88, v89\n"
    "  s_waitcnt lgkmcnt(2)\n"
    "  v_mfma_f32_32x32x16_bf16 v[96:111], v[224:227], v[40:43], v[96:111]\n"
    "  ds_read_b128 v[240:243], v3 offset:32768\n"
    "  v_max3_f32 v244, v244, v90, v91\n"
    "  v_max3_f32 v244, v244, v92, v93\n"
    "  v_mfma_f32_32x32x16_bf16 v[96:111], v[228:231], v[44:47], v[96:111]\n"
    "  ds_read_b128 v[224:227], v4 offset:32768\n"
    "  v_max3_f32 v244, v244, v94, v95\n"
    "  s_waitcnt lgkmcnt(2)\n"
    "  v_mfma_f32_32x32x16_bf16 v[112:127], v[232:235], v[16:19], 0\n"
    "  ds_read_b128 v[228:231], v1 offset:33792\n"
    "  v_mfma_f32_32x32x16_bf16 v[112:127], v[236:239], v[20:23], v[112:127]\n"
    "  ds_read_b128 v[232:235], v2 offset:33792\n"
    "  s_waitcnt lgkmcnt(2)\n"
    "  v_mfma_f32_32x32x16_bf16 v[112:127], v[240:243], v[24:27], v[112:127]\n"
    "  ds_read_b128 v[236:239], v3 offset:33792\n"
    "  v_mfma_f32_32x32x16_bf16 v[112:127], v[224:227], v[28:31], v[112:127]\n"
    "  ds_read_b128 v[240:243], v4 offset:33792\n"
    "  v_max3_f32 v244, v244, v96, v97\n"
    "  v_max3_f32 v244, v244, v98, v99\n"
    "  s_cmp_gt_u32 s26, 3\n"
    "  s_cbranch_scc1 .Lgqa_sk40\n"
    "  s_barrier\n"
    ".Lgqa_sk40:\n"
    "  s_waitcnt lgkmcnt(2)\n"
    "  v_mfma_f32_32x32x16_bf16 v[112:127], v[228:231], v[32:35], v[112:127]\n"
    "  v_add_u32 v1, 0xa000, v1\n"
    "  v_add_u32 v2, 0xa000, v2\n"
    "  v_add_u32 v3, 0xa000, v3\n"
    "  v_add_u32 v4, 0xa000, v4\n"
    "  ds_read_b128 v[224:227], v1 offset:0\n"
    "  v_max3_f32 v244, v244, v100, v101\n"
    "  v_max3_f32 v244, v244, v102, v103\n"
    "  v_mfma_f32_32x32x16_bf16 v[112:127], v[232:235], v[36:39], v[112:127]\n"
    "  ds_read_b128 v[228:231], v2 offset:0\n"
    "  v_max3_f32 v244, v244, v104, v105\n"
    "  s_waitcnt lgkmcnt(2)\n"
    "  v_mfma_f32_32x32x16_bf16 v[112:127], v[236:239], v[40:43], v[112:127]\n"
    "  ds_read_b128 v[232:235], v3 offset:0\n"
    "  v_max3_f32 v244, v244, v106, v107\n"
    "  v_max3_f32 v244, v244, v108, v109\n"
    "  v_mfma_f32_32x32x16_bf16 v[112:127], v[240:243], v[44:47], v[112:127]\n"
    "  ds_read_b128 v[236:239], v4 offset:0\n"
    "  v_max3_f32 v244, v244, v110, v111\n"
    "  s_waitcnt lgkmcnt(2)\n"
    "  v_mfma_f32_32x32x16_bf16 v[128:143], v[224:227], v[16:19], 0\n"
    "  ds_read_b128 v[240:243], v1 offset:1024\n"
    "  v_mfma_f32_32x32x16_bf16 v[128:143], v[228:231], v[20:23], v[128:143]\n"
    "  ds_read_b128 v[224:227], v2 offset:1024\n"
    "  s_waitcnt lgkmcnt(2)\n"
    "  v_mfma_f32_32x32x16_bf16 v[128:143], v[232:235], v[24:27], v[128:143]\n"
    "  ds_read_b128 v[228:231], v3 offset:1024\n"
    "  v_mfma_f32_32x32x16_bf16 v[128:143], v[236:239], v[28:31], v[128:143]\n"
    "  ds_read_b128 v[232:235], v4 offset:1024\n"
    "  v_max3_f32 v244, v244, v112, v113\n"
    "  v_max3_f32 v244, v244, v114, v115\n"
    "  s_waitcnt lgkmcnt(2)\n"
    "  v_mfma_f32_32x32x16_bf16 v[128:143], v[240:243], v[32:35], v[128:143]\n"
    "  ds_read_b128 v[236:239], v1 offset:8192\n"
    "  v_max3_f32 v244, v244, v116, v117\n"
    "  v_max3_f32 v244, v244, v118, v119\n"
    "  v_mfma_f32_32x32x16_bf16 v[128:143], v[224:227], v[36:39], v[128:143]\n"
    "  ds_read_b128 v[240:243], v2 offset:8192\n"
    "  v_max3_f32 v244, v244, v120, v121\n"
    "  s_waitcnt lgkmcnt(2)\n"
    "  v_mfma_f32_32x32x16_bf16 v[128:143], v[228:231], v[40:43], v[128:143]\n"
    "  ds_read_b128 v[224:227], v3 offset:8192\n"
    "  v_max3_f32 v244, v244, v122, v123\n"
    "  v_max3_f32 v244, v244, v124, v125\n"
    "  v_mfma_f32_32x32x16_bf16 v[128:143], v[232:235], v[44:47], v[128:143]\n"
    "  ds_read_b128 v[228:231], v4 offset:8192\n"
    "  v_max3_f32 v244, v244, v126, v127\n"
    "  s_waitcnt lgkmcnt(2)\n"
    "  v_mfma_f32_32x32x16_bf16 v[144:159], v[236:239], v[16:19], 0\n"
    "  ds_read_b128 v[232:235], v1 offset:9216\n"
    "  v_mfma_f32_32x32x16_bf16 v[144:159], v[240:243], v[20:23], v[144:159]\n"
    "  ds_read_b128 v[236:239], v2 offset:9216\n"
    "  s_waitcnt lgkmcnt(2)\n"
    "  v_mfma_f32_32x32x16_bf16 v[144:159], v[224:227], v[24:27], v[144:159]\n"
    "  ds_read_b128 v[240:243], v3 offset:9216\n"
    "  v_mfma_f32_32x32x16_bf16 v[144:159], v[228:231], v[28:31], v[144:159]\n"
    "  ds_read_b128 v[224:227], v4 offset:9216\n"
    "  v_max3_f32 v244, v244, v128, v129\n"
    "  v_max3_f32 v244, v244, v130, v131\n"
    "  s_waitcnt lgkmcnt(2)\n"
    "  v_mfma_f32_32x32x16_bf16 v[144:159], v[232:235], v[32:35], v[144:159]\n"
    "  ds_read_b128 v[228:231], v1 offset:16384\n"
    "  v_max3_f32 v244, v244, v132, v133\n"
    "  v_max3_f32 v244, v244, v134, v135\n"
    "  v_mfma_f32_32x32x16_bf16 v[144:159], v[236:239], v[36:39], v[144:159]\n"
    "  ds_read_b128 v[232:235], v2 offset:16384\n"
    "  v_max3_f32 v244, v244, v136, v137\n"
    "  s_waitcnt lgkmcnt(2)\n"
    "  v_mfma_f32_32x32x16_bf16 v[144:159], v[240:243], v[40:43], v[144:159]\n"
    "  ds_read_b128 v[236:239], v3 offset:16384\n"
    "  v_max3_f32 v244, v244, v138, v139\n"
    "  v_max3_f32 v244, v244, v140, v141\n"
    "  v_mfma_f32_32x32x16_bf16 v[144:159], v[224:227], v[44:47], v[144:159]\n"
    "  ds_read_b128 v[240:243], v4 offset:16384\n"
    "  v_max3_f32 v244, v244, v142, v143\n"
    "  s_waitcnt lgkmcnt(2)\n"
    "  v_mfma_f32_32x32x16_bf16 v[160:175], v[228:231], v[16:19], 0\n"
    "  ds_read_b128 v[224:227], v1 offset:17408\n"
    "  v_mfma_f32_32x32x16_bf16 v[160:175], v[232:235], v[20:23], v[160:175]\n"
    "  ds_read_b128 v[228:231], v2 offset:17408\n"
    "  s_waitcnt lgkmcnt(2)\n"
    "  v_mfma_f32_32x32x16_bf16 v[160:175], v[236:239], v[24:27], v[160:175]\n"
    "  ds_read_b128 v[232:235], v3 offset:17408\n"
    "  v_mfma_f32_32x32x16_bf16 v[160:175], v[240:243], v[28:31], v[160:175]\n"
    "  ds_read_b128 v[236:239], v4 offset:17408\n"
    "  v_max3_f32 v244, v244, v144, v145\n"
    "  v_max3_f32 v244, v244, v146, v147\n"
    "  s_waitcnt lgkmcnt(2)\n"
    "  v_mfma_f32_32x32x16_bf16 v[160:175], v[224:227], v[32:35], v[160:175]\n"
    "  ds_read_b128 v[240:243], v1 offset:24576\n"
    "  v_max3_f32 v244, v244, v148, v149\n"
    "  v_max3_f32 v244, v244, v150, v151\n"
    "  v_mfma_f32_32x32x16_bf16 v[160:175], v[228:231], v[36:39], v[160:175]\n"
    "  ds_read_b128 v[224:227], v2 offset:24576\n"
    "  v_max3_f32 v244, v244, v152, v153\n"
    "  s_waitcnt lgkmcnt(2)\n"
    "  v_mfma_f32_32x32x16_bf16 v[160:175], v[232:235], v[40:43], v[160:175]\n"
    "  ds_read_b128 v[228:231], v3 offset:24576\n"
    "  v_max3_f32 v244, v244, v154, v155\n"
    "  v_max3_f32 v244, v244, v156, v157\n"
    "  v_mfma_f32_32x32x16_bf16 v[160:175], v[236:239], v[44:47], v[160:175]\n"
    "  ds_read_b128 v[232:235], v4 offset:24576\n"
    "  v_max3_f32 v244, v244, v158, v159\n"
    "  s_waitcnt lgkmcnt(2)\n"
    "  v_mfma_f32_32x32x16_bf16 v[176:191], v[240:243], v[16:19], 0\n"
    "  ds_read_b128 v[236:239], v1 offset:25600\n"
    "  v_mfma_f32_32x32x16_bf16 v[176:191], v[224:227], v[20:23], v[176:191]\n"
    "  ds_read_b128 v[240:243], v2 offset:25600\n"
    "  s_waitcnt lgkmcnt(2)\n"
    "  v_mfma_f32_32x32x16_bf16 v[176:191], v[228:231], v[24:27], v[176:191]\n"
    "  ds_read_b128 v[224:227], v3 offset:25600\n"
    "  v_mfma_f32_32x32x16_bf16 v[176:191], v[232:235], v[28:31], v[176:191]\n"
    "  ds_read_b128 v[228:231], v4 offset:25600\n"
    "  v_max3_f32 v244, v244, v160, v161\n"
    "  v_max3_f32 v244, v244, v162, v163\n"
    "  s_waitcnt lgkmcnt(2)\n"
    "  v_mfma_f32_32x32x16_bf16 v[176:191], v[236:239], v[32:35], v[176:191]\n"
    "  ds_read_b128 v[232:235], v1 offset:32768\n"
    "  v_max3_f32 v244, v244, v164, v165\n"
    "  v_max3_f32 v244, v244, v166, v167\n"
    "  v_mfma_f32_32x32x16_bf16 v[176:191], v[240:243], v[36:39], v[176:191]\n"
    "  ds_read_b128 v[236:239], v2 offset:32768\n"
    "  v_max3_f32 v244, v244, v168, v169\n"
    "  s_waitcnt lgkmcnt(2)\n"
    "  v_mfma_f32_32x32x16_bf16 v[176:191], v[224:227], v[40:43], v[176:191]\n"
    "  ds_read_b128 v[240:243], v3 offset:32768\n"
    "  v_max3_f32 v244, v244, v170, v171\n"
    "  v_max3_f32 v244, v244, v172, v173\n"
    "  v_mfma_f32_32x32x16_bf16 v[176:191], v[228:231], v[44:47], v[176:191]\n"
    "  ds_read_b128 v[224:227], v4 offset:32768\n"
    "  v_max3_f32 v244, v244, v174, v175\n"
    "  s_waitcnt lgkmcnt(2)\n"
    "  v_mfma_f32_32x32x16_bf16 v[192:207], v[232:235], v[16:19], 0\n"
    "  ds_read_b128 v[228:231], v1 offset:33792\n"
    "  v_mfma_f32_32x32x16_bf16 v[192:207], v[236:239], v[20:23], v[192:207]\n"
    "  ds_read_b128 v[232:235], v2 offset:33792\n"
    "  s_waitcnt lgkmcnt(2)\n"
    "  v_mfma_f32_32x32x16_bf16 v[192:207], v[240:243], v[24:27], v[192:207]\n"
    "  ds_read_b128 v[236:239], v3 offset:33792\n"
    "  v_mfma_f32_32x32x16_bf16 v[192:207], v[224:227], v[28:31], v[192:207]\n"
    "  ds_read_b128 v[240:243], v4 offset:33792\n"
    "  v_max3_f32 v244, v244, v176, v177\n"
    "  v_max3_f32 v244, v244, v178, v179\n"
    "  s_waitcnt lgkmcnt(2)\n"
    "  v_mfma_f32_32x32x16_bf16 v[192:207], v[228:231], v[32:35], v[192:207]\n"
    "  v_max3_f32 v244, v244, v180, v181\n"
    "  v_max3_f32 v244, v244, v182, v183\n"
    "  v_mfma_f32_32x32x16_bf16 v[192:207], v[232:235], v[36:39], v[192:207]\n"
    "  v_max3_f32 v244, v244, v184, v185\n"
    "  s_waitcnt lgkmcnt(0)\n"
    "  v_mfma_f32_32x32x16_bf16 v[192:207], v[236:239], v[40:43], v[192:207]\n"
    "  v_max3_f32 v244, v244, v186, v187\n"
    "  v_max3_f32 v244, v244, v188, v189\n"
    "  v_mfma_f32_32x32x16_bf16 v[192:207], v[240:243], v[44:47], v[192:207]\n"
    "  v_max3_f32 v244, v244, v190, v191\n"
    "  s_setprio 0\n"
    "  s_cmp_gt_u32 s26, 3\n"
    "  s_cbranch_scc1 .Lgqa_prh\n"
    "  s_setprio 1\n"
    ".Lgqa_prh:\n"
    "  s_cmp_lt_u32 s26, 4\n"
    "  s_cbranch_scc1 .Lgqa_bkd\n"
    "  s_lshl_b32 s77, s26, 2\n"
    "  v_mov_b32 v250, 1\n"
    "  v_lshlrev_b32 v251, 10, v13\n"
    "  buffer_store_dword v250, v251, s[20:23], s77 offen offset:32\n"
    ".Lgqa_bkd:\n"
    "  s_nop 5\n"
    "  v_max3_f32 v244, v244, v192, v193\n"
    "  v_max3_f32 v244, v244, v194, v195\n"
    "  v_max3_f32 v244, v244, v196, v197\n"
    "  v_max3_f32 v244, v244, v198, v199\n"
    "  v_max3_f32 v244, v244, v200, v201\n"
    "  v_max3_f32 v244, v244, v202, v203\n"
    "  v_max3_f32 v244, v244, v204, v205\n"
    "  v_max3_f32 v244, v244, v206, v207\n"
    "  v_mov_b32 v250, v244\n"
    "  s_nop 1\n"
    "  v_permlane32_swap_b32 v250, v244\n"
    "  v_max_f32 v244, v244, v250\n"
    "  v_mul_f32 v247, 0x3e0293ee, v244\n"
    "  v_sub_f32 v247, 0, v247\n"
    "  v_fma_f32 v250, v48, v248, v247\n"
    "  v_fma_f32 v251, v49, v248, v247\n"
    "  v_fma_f32 v252, v50, v248, v247\n"
    "  v_fma_f32 v253, v51, v248, v247\n"
    "  v_exp_f32 v250, v250\n"
    "  v_exp_f32 v251, v251\n"
    "  v_fma_f32 v254, v52, v248, v247\n"
    "  v_fma_f32 v255, v53, v248, v247\n"
    "  v_add_f32 v246, v246, v250\n"
    "  v_cvt_pk_bf16_f32 v208, v250, v251\n"
    "  v_add_f32 v246, v246, v251\n"
    "  v_exp_f32 v252, v252\n"
    "  v_exp_f32 v253, v253\n"
    "  v_fma_f32 v250, v54, v248, v247\n"
    "  v_fma_f32 v251, v55, v248, v247\n"
    "  v_add_f32 v245, v245, v252\n"
    "  v_cvt_pk_bf16_f32 v209, v252, v253\n"
    "  v_add_f32 v245, v245, v253\n"
    "  v_exp_f32 v254, v254\n"
    "  v_exp_f32 v255, v255\n"
    "  v_fma_f32 v252, v56, v248, v247\n"
    "  v_fma_f32 v253, v57, v248, v247\n"
    "  v_add_f32 v246, v246, v254\n"
    "  v_cvt_pk_bf16_f32 v210, v254, v255\n"
    "  v_add_f32 v246, v246, v255\n"
    "  v_exp_f32 v250, v250\n"
    "  v_exp_f32 v251, v251\n"
    "  v_fma_f32 v254, v58, v248, v247\n"
    "  v_fma_f32 v255, v59, v248, v247\n"
    "  v_add_f32 v245, v245, v250\n"
    "  v_cvt_pk_bf16_f32 v211, v250, v251\n"
    "  v_add_f32 v245, v245, v251\n"
    "  v_exp_f32 v252, v252\n"
    "  v_exp_f32 v253, v253\n"
    "  v_fma_f32 v250, v60, v248, v247\n"
    "  v_fma_f32 v251, v61, v248, v247\n"
    "  v_add_f32 v246, v246, v252\n"
    "  v_cvt_pk_bf16_f32 v212, v252, v253\n"
    "  v_add_f32 v246, v246, v253\n"
    "  v_exp_f32 v254, v254\n"
    "  v_exp_f32 v255, v255\n"
    "  v_fma_f32 v252, v62, v248, v247\n"
    "  v_fma_f32 v253, v63, v248, v247\n"
    "  v_add_f32 v245, v245, v254\n"
    "  v_cvt_pk_bf16_f32 v213, v254, v255\n"
    "  v_add_f32 v245, v245, v255\n"
    "  v_exp_f32 v250, v250\n"
    "  v_exp_f32 v251, v251\n"
    "  v_fma_f32 v254, v64, v248, v247\n"
    "  v_fma_f32 v255, v65, v248, v247\n"
    "  v_add_f32 v246, v246, v250\n"
    "  v_cvt_pk_bf16_f32 v214, v250, v251\n"
    "  v_add_f32 v246, v246, v251\n"
    "  v_exp_f32 v252, v252\n"
    "  v_exp_f32 v253, v253\n"
    "  v_fma_f32 v250, v66, v248, v247\n"
    "  v_fma_f32 v251, v67, v248, v247\n"
    "  v_add_f32 v245, v245, v252\n"
    "  v_cvt_pk_bf16_f32 v215, v252, v253\n"
    "  v_add_f32 v245, v245, v253\n"
    "  v_exp_f32 v254, v254\n"
    "  v_exp_f32 v255, v255\n"
    "  v_fma_f32 v252, v68, v248, v247\n"
    "  v_fma_f32 v253, v69, v248, v247\n"
    "  v_add_f32 v246, v246, v254\n"
    "  v_cvt_pk_bf16_f32 v48, v254, v255\n"
    "  v_add_f32 v246, v246, v255\n"
    "  v_exp_f32 v250, v250\n"
    "  v_exp_f32 v251, v251\n"
    "  v_fma_f32 v254, v70, v248, v247\n"
    "  v_fma_f32 v255, v71, v248, v247\n"
    "  v_add_f32 v245, v245, v250\n"
    "  v_cvt_pk_bf16_f32 v49, v250, v251\n"
    "  v_add_f32 v245, v245, v251\n"
    "  v_exp_f32 v252, v252\n"
    "  v_exp_f32 v253, v253\n"
    "  v_fma_f32 v250, v72, v248, v247\n"
    "  v_fma_f32 v251, v73, v248, v247\n"
    "  v_add_f32 v246, v246, v252\n"
    "  v_cvt_pk_bf16_f32 v50, v252, v253\n"
    "  v_add_f32 v246, v246, v253\n"
    "  v_exp_f32 v254, v254\n"
    "  v_exp_f32 v255, v255\n"
    "  v_fma_f32 v252, v74, v248, v247\n"
    "  v_fma_f32 v253, v75, v248, v247\n"
    "  v_add_f32 v245, v245, v254\n"
    "  v_cvt_pk_bf16_f32 v51, v254, v255\n"
    "  v_add_f32 v245, v245, v255\n"
    "  v_exp_f32 v250, v250\n"
    "  v_exp_f32 v251, v251\n"
    "  v_fma_f32 v254, v76, v248, v247\n"
    "  v_fma_f32 v255, v77, v248, v247\n"
    "  v_add_f32 v246, v246, v250\n"
    "  v_cvt_pk_bf16_f32 v52, v250, v251\n"
    "  v_add_f32 v246, v246, v251\n"
    "  v_exp_f32 v252, v252\n"
    "  v_exp_f32 v253, v253\n"
    "  v_fma_f32 v250, v78, v248, v247\n"
    "  v_fma_f32 v251, v79, v248, v247\n"
    "  v_add_f32 v245, v245, v252\n"
    "  v_cvt_pk_bf16_f32 v53, v252, v253\n"
    "  v_add_f32 v245, v245, v253\n"
    "  v_exp_f32 v254, v254\n"
    "  v_exp_f32 v255, v255\n"
    "  v_fma_f32 v252, v80, v248, v247\n"
    "  v_fma_f32 v253, v81, v248, v247\n"
    "  v_add_f32 v246, v246, v254\n"
    "  v_cvt_pk_bf16_f32 v54, v254, v255\n"
    "  v_add_f32 v246, v246, v255\n"
    "  v_exp_f32 v250, v250\n"
    "  v_exp_f32 v251, v251\n"
    "  v_fma_f32 v254, v82, v248, v247\n"
    "  v_fma_f32 v255, v83, v248, v247\n"
    "  v_add_f32 v245, v245, v250\n"
    "  v_cvt_pk_bf16_f32 v55, v250, v251\n"
    "  v_add_f32 v245, v245, v251\n"
    "  v_exp_f32 v252, v252\n"
    "  v_exp_f32 v253, v253\n"
    "  v_fma_f32 v250, v84, v248, v247\n"
    "  v_fma_f32 v251, v85, v248, v247\n"
    "  v_add_f32 v246, v246, v252\n"
    "  v_cvt_pk_bf16_f32 v56, v252, v253\n"
    "  v_add_f32 v246, v246, v253\n"
    "  v_exp_f32 v254, v254\n"
    "  v_exp_f32 v255, v255\n"
    "  v_fma_f32 v252, v86, v248, v247\n"
    "  v_fma_f32 v253, v87, v248, v247\n"
    "  v_add_f32 v245, v245, v254\n"
    "  v_cvt_pk_bf16_f32 v57, v254, v255\n"
    "  v_add_f32 v245, v245, v255\n"
    "  v_exp_f32 v250, v250\n"
    "  v_exp_f32 v251, v251\n"
    "  v_fma_f32 v254, v88, v248, v247\n"
    "  v_fma_f32 v255, v89, v248, v247\n"
    "  v_add_f32 v246, v246, v250\n"
    "  v_cvt_pk_bf16_f32 v58, v250, v251\n"
    "  v_add_f32 v246, v246, v251\n"
    "  v_exp_f32 v252, v252\n"
    "  v_exp_f32 v253, v253\n"
    "  v_fma_f32 v250, v90, v248, v247\n"
    "  v_fma_f32 v251, v91, v248, v247\n"
    "  v_add_f32 v245, v245, v252\n"
    "  v_cvt_pk_bf16_f32 v59, v252, v253\n"
    "  v_add_f32 v245, v245, v253\n"
    "  v_exp_f32 v254, v254\n"
    "  v_exp_f32 v255, v255\n"
    "  v_fma_f32 v252, v92, v248, v247\n"
    "  v_fma_f32 v253, v93, v248, v247\n"
    "  v_add_f32 v246, v246, v254\n"
    "  v_cvt_pk_bf16_f32 v60, v254, v255\n"
    "  v_add_f32 v246, v246, v255\n"
    "  v_exp_f32 v250, v250\n"
    "  v_exp_f32 v251, v251\n"
    "  v_fma_f32 v254, v94, v248, v247\n"
    "  v_fma_f32 v255, v95, v248, v247\n"
    "  v_add_f32 v245, v245, v250\n"
    "  v_cvt_pk_bf16_f32 v61, v250, v251\n"
    "  v_add_f32 v245, v245, v251\n"
    "  v_exp_f32 v252, v252\n"
    "  v_exp_f32 v253, v253\n"
    "  v_fma_f32 v250, v96, v248, v247\n"
    "  v_fma_f32 v251, v97, v248, v247\n"
    "  v_add_f32 v246, v246, v252\n"
    "  v_cvt_pk_bf16_f32 v62, v252, v253\n"
    "  v_add_f32 v246, v246, v253\n"
    "  v_exp_f32 v254, v254\n"
    "  v_exp_f32 v255, v255\n"
    "  v_fma_f32 v252, v98, v248, v247\n"
    "  v_fma_f32 v253, v99, v248, v247\n"
    "  v_add_f32 v245, v245, v254\n"
    "  v_cvt_pk_bf16_f32 v63, v254, v255\n"
    "  v_add_f32 v245, v245, v255\n"
    "  v_exp_f32 v250, v250\n"
    "  v_exp_f32 v251, v251\n"
    "  v_fma_f32 v254, v100, v248, v247\n"
    "  v_fma_f32 v255, v101, v248, v247\n"
    "  v_add_f32 v246, v246, v250\n"
    "  v_cvt_pk_bf16_f32 v64, v250, v251\n"
    "  v_add_f32 v246, v246, v251\n"
    "  v_exp_f32 v252, v252\n"
    "  v_exp_f32 v253, v253\n"
    "  v_fma_f32 v250, v102, v248, v247\n"
    "  v_fma_f32 v251, v103, v248, v247\n"
    "  v_add_f32 v245, v245, v252\n"
    "  v_cvt_pk_bf16_f32 v65, v252, v253\n"
    "  v_add_f32 v245, v245, v253\n"
    "  v_exp_f32 v254, v254\n"
    "  v_exp_f32 v255, v255\n"
    "  v_fma_f32 v252, v104, v248, v247\n"
    "  v_fma_f32 v253, v105, v248, v247\n"
    "  v_add_f32 v246, v246, v254\n"
    "  v_cvt_pk_bf16_f32 v66, v254, v255\n"
    "  v_add_f32 v246, v246, v255\n"
    "  v_exp_f32 v250, v250\n"
    "  v_exp_f32 v251, v251\n"
    "  v_fma_f32 v254, v106, v248, v247\n"
    "  v_fma_f32 v255, v107, v248, v247\n"
    "  v_add_f32 v245, v245, v250\n"
    "  v_cvt_pk_bf16_f32 v67, v250, v251\n"
    "  v_add_f32 v245, v245, v251\n"
    "  v_exp_f32 v252, v252\n"
    "  v_exp_f32 v253, v253\n"
    "  v_fma_f32 v250, v108, v248, v247\n"
    "  v_fma_f32 v251, v109, v248, v247\n"
    "  v_add_f32 v246, v246, v252\n"
    "  v_cvt_pk_bf16_f32 v68, v252, v253\n"
    "  v_add_f32 v246, v246, v253\n"
    "  v_exp_f32 v254, v254\n"
    "  v_exp_f32 v255, v255\n"
    "  v_fma_f32 v252, v110, v248, v247\n"
    "  v_fma_f32 v253, v111, v248, v247\n"
    "  v_add_f32 v245, v245, v254\n"
    "  v_cvt_pk_bf16_f32 v69, v254, v255\n"
    "  v_add_f32 v245, v245, v255\n"
    "  v_exp_f32 v250, v250\n"
    "  v_exp_f32 v251, v251\n"
    "  v_fma_f32 v254, v112, v248, v247\n"
    "  v_fma_f32 v255, v113, v248, v247\n"
    "  v_add_f32 v246, v246, v250\n"
    "  v_cvt_pk_bf16_f32 v70, v250, v251\n"
    "  v_add_f32 v246, v246, v251\n"
    "  v_exp_f32 v252, v252\n"
    "  v_exp_f32 v253, v253\n"
    "  v_fma_f32 v250, v114, v248, v247\n"
    "  v_fma_f32 v251, v115, v248, v247\n"
    "  v_add_f32 v245, v245, v252\n"
    "  v_cvt_pk_bf16_f32 v71, v252, v253\n"
    "  v_add_f32 v245, v245, v253\n"
    "  v_exp_f32 v254, v254\n"
    "  v_exp_f32 v255, v255\n"
    "  v_fma_f32 v252, v116, v248, v247\n"
    "  v_fma_f32 v253, v117, v248, v247\n"
    "  v_add_f32 v246, v246, v254\n"
    "  v_cvt_pk_bf16_f32 v72, v254, v255\n"
    "  v_add_f32 v246, v246, v255\n"
    "  v_exp_f32 v250, v250\n"
    "  v_exp_f32 v251, v251\n"
    "  v_fma_f32 v254, v118, v248, v247\n"
    "  v_fma_f32 v255, v119, v248, v247\n"
    "  v_add_f32 v245, v245, v250\n"
    "  v_cvt_pk_bf16_f32 v73, v250, v251\n"
    "  v_add_f32 v245, v245, v251\n"
    "  v_exp_f32 v252, v252\n"
    "  v_exp_f32 v253, v253\n"
    "  v_fma_f32 v250, v120, v248, v247\n"
    "  v_fma_f32 v251, v121, v248, v247\n"
    "  v_add_f32 v246, v246, v252\n"
    "  v_cvt_pk_bf16_f32 v74, v252, v253\n"
    "  v_add_f32 v246, v246, v253\n"
    "  v_exp_f32 v254, v254\n"
    "  v_exp_f32 v255, v255\n"
    "  v_fma_f32 v252, v122, v248, v247\n"
    "  v_fma_f32 v253, v123, v248, v247\n"
    "  v_add_f32 v245, v245, v254\n"
    "  v_cvt_pk_bf16_f32 v75, v254, v255\n"
    "  v_add_f32 v245, v245, v255\n"
    "  v_exp_f32 v250, v250\n"
    "  v_exp_f32 v251, v251\n"
    "  v_fma_f32 v254, v124, v248, v247\n"
    "  v_fma_f32 v255, v125, v248, v247\n"
    "  v_add_f32 v246, v246, v250\n"
    "  v_cvt_pk_bf16_f32 v76, v250, v251\n"
    "  v_add_f32 v246, v246, v251\n"
    "  v_exp_f32 v252, v252\n"
    "  v_exp_f32 v253, v253\n"
    "  v_fma_f32 v250, v126, v248, v247\n"
    "  v_fma_f32 v251, v127, v248, v247\n"
    "  v_add_f32 v245, v245, v252\n"
    "  v_cvt_pk_bf16_f32 v77, v252, v253\n"
    "  v_add_f32 v245, v245, v253\n"
    "  v_exp_f32 v254, v254\n"
    "  v_exp_f32 v255, v255\n"
    "  v_fma_f32 v252, v128, v248, v247\n"
    "  v_fma_f32 v253, v129, v248, v247\n"
    "  v_add_f32 v246, v246, v254\n"
    "  v_cvt_pk_bf16_f32 v78, v254, v255\n"
    "  v_add_f32 v246, v246, v255\n"
    "  v_exp_f32 v250, v250\n"
    "  v_exp_f32 v251, v251\n"
    "  v_fma_f32 v254, v130, v248, v247\n"
    "  v_fma_f32 v255, v131, v248, v247\n"
    "  v_add_f32 v245, v245, v250\n"
    "  v_cvt_pk_bf16_f32 v79, v250, v251\n"
    "  v_add_f32 v245, v245, v251\n"
    "  v_exp_f32 v252, v252\n"
    "  v_exp_f32 v253, v253\n"
    "  v_fma_f32 v250, v132, v248, v247\n"
    "  v_fma_f32 v251, v133, v248, v247\n"
    "  v_add_f32 v246, v246, v252\n"
    "  v_cvt_pk_bf16_f32 v216, v252, v253\n"
    "  v_add_f32 v246, v246, v253\n"
    "  v_exp_f32 v254, v254\n"
    "  v_exp_f32 v255, v255\n"
    "  v_fma_f32 v252, v134, v248, v247\n"
    "  v_fma_f32 v253, v135, v248, v247\n"
    "  v_add_f32 v245, v245, v254\n"
    "  v_cvt_pk_bf16_f32 v217, v254, v255\n"
    "  v_add_f32 v245, v245, v255\n"
    "  v_exp_f32 v250, v250\n"
    "  v_exp_f32 v251, v251\n"
    "  v_fma_f32 v254, v136, v248, v247\n"
    "  v_fma_f32 v255, v137, v248, v247\n"
    "  v_add_f32 v246, v246, v250\n"
    "  v_cvt_pk_bf16_f32 v218, v250, v251\n"
    "  v_add_f32 v246, v246, v251\n"
    "  v_exp_f32 v252, v252\n"
    "  v_exp_f32 v253, v253\n"
    "  v_fma_f32 v250, v138, v248, v247\n"
    "  v_fma_f32 v251, v139, v248, v247\n"
    "  v_add_f32 v245, v245, v252\n"
    "  v_cvt_pk_bf16_f32 v219, v252, v253\n"
    "  v_add_f32 v245, v245, v253\n"
    "  v_exp_f32 v254, v254\n"
    "  v_exp_f32 v255, v255\n"
    "  v_fma_f32 v252, v140, v248, v247\n"
    "  v_fma_f32 v253, v141, v248, v247\n"
    "  v_add_f32 v246, v246, v254\n"
    "  v_cvt_pk_bf16_f32 v220, v254, v255\n"
    "  v_add_f32 v246, v246, v255\n"
    "  v_exp_f32 v250, v250\n"
    "  v_exp_f32 v251, v251\n"
    "  v_fma_f32 v254, v142, v248, v247\n"
    "  v_fma_f32 v255, v143, v248, v247\n"
    "  v_add_f32 v245, v245, v250\n"
    "  v_cvt_pk_bf16_f32 v221, v250, v251\n"
    "  v_add_f32 v245, v245, v251\n"
    "  v_exp_f32 v252, v252\n"
    "  v_exp_f32 v253, v253\n"
    "  v_fma_f32 v250, v144, v248, v247\n"
    "  v_fma_f32 v251, v145, v248, v247\n"
    "  v_add_f32 v246, v246, v252\n"
    "  v_cvt_pk_bf16_f32 v222, v252, v253\n"
    "  v_add_f32 v246, v246, v253\n"
    "  v_exp_f32 v254, v254\n"
    "  v_exp_f32 v255, v255\n"
    "  v_fma_f32 v252, v146, v248, v247\n"
    "  v_fma_f32 v253, v147, v248, v247\n"
    "  v_add_f32 v245, v245, v254\n"
    "  v_cvt_pk_bf16_f32 v223, v254, v255\n"
    "  v_add_f32 v245, v245, v255\n"
    "  v_exp_f32 v250, v250\n"
    "  v_exp_f32 v251, v251\n"
    "  v_fma_f32 v254, v148, v248, v247\n"
    "  v_fma_f32 v255, v149, v248, v247\n"
    "  v_add_f32 v246, v246, v250\n"
    "  v_cvt_pk_bf16_f32 v128, v250, v251\n"
    "  v_add_f32 v246, v246, v251\n"
    "  v_exp_f32 v252, v252\n"
    "  v_exp_f32 v253, v253\n"
    "  v_fma_f32 v250, v150, v248, v247\n"
    "  v_fma_f32 v251, v151, v248, v247\n"
    "  v_add_f32 v245, v245, v252\n"
    "  v_cvt_pk_bf16_f32 v129, v252, v253\n"
    "  v_add_f32 v245, v245, v253\n"
    "  v_exp_f32 v254, v254\n"
    "  v_exp_f32 v255, v255\n"
    "  v_fma_f32 v252, v152, v248, v247\n"
    "  v_fma_f32 v253, v153, v248, v247\n"
    "  v_add_f32 v246, v246, v254\n"
    "  v_cvt_pk_bf16_f32 v130, v254, v255\n"
    "  v_add_f32 v246, v246, v255\n"
    "  v_exp_f32 v250, v250\n"
    "  v_exp_f32 v251, v251\n"
    "  v_fma_f32 v254, v154, v248, v247\n"
    "  v_fma_f32 v255, v155, v248, v247\n"
    "  v_add_f32 v245, v245, v250\n"
    "  v_cvt_pk_bf16_f32 v131, v250, v251\n"
    "  v_add_f32 v245, v245, v251\n"
    "  v_exp_f32 v252, v252\n"
    "  v_exp_f32 v253, v253\n"
    "  v_fma_f32 v250, v156, v248, v247\n"
    "  v_fma_f32 v251, v157, v248, v247\n"
    "  v_add_f32 v246, v246, v252\n"
    "  v_cvt_pk_bf16_f32 v132, v252, v253\n"
    "  v_add_f32 v246, v246, v253\n"
    "  v_exp_f32 v254, v254\n"
    "  v_exp_f32 v255, v255\n"
    "  v_fma_f32 v252, v158, v248, v247\n"
    "  v_fma_f32 v253, v159, v248, v247\n"
    "  v_add_f32 v245, v245, v254\n"
    "  v_cvt_pk_bf16_f32 v133, v254, v255\n"
    "  v_add_f32 v245, v245, v255\n"
    "  v_exp_f32 v250, v250\n"
    "  v_exp_f32 v251, v251\n"
    "  v_fma_f32 v254, v160, v248, v247\n"
    "  v_fma_f32 v255, v161, v248, v247\n"
    "  v_add_f32 v246, v246, v250\n"
    "  v_cvt_pk_bf16_f32 v134, v250, v251\n"
    "  v_add_f32 v246, v246, v251\n"
    "  v_exp_f32 v252, v252\n"
    "  v_exp_f32 v253, v253\n"
    "  v_fma_f32 v250, v162, v248, v247\n"
    "  v_fma_f32 v251, v163, v248, v247\n"
    "  v_add_f32 v245, v245, v252\n"
    "  v_cvt_pk_bf16_f32 v135, v252, v253\n"
    "  v_add_f32 v245, v245, v253\n"
    "  v_exp_f32 v254, v254\n"
    "  v_exp_f32 v255, v255\n"
    "  v_fma_f32 v252, v164, v248, v247\n"
    "  v_fma_f32 v253, v165, v248, v247\n"
    "  v_add_f32 v246, v246, v254\n"
    "  v_cvt_pk_bf16_f32 v136, v254, v255\n"
    "  v_add_f32 v246, v246, v255\n"
    "  v_exp_f32 v250, v250\n"
    "  v_exp_f32 v251, v251\n"
    "  v_fma_f32 v254, v166, v248, v247\n"
    "  v_fma_f32 v255, v167, v248, v247\n"
    "  v_add_f32 v245, v245, v250\n"
    "  v_cvt_pk_bf16_f32 v137, v250, v251\n"
    "  v_add_f32 v245, v245, v251\n"
    "  v_exp_f32 v252, v252\n"
    "  v_exp_f32 v253, v253\n"
    "  v_fma_f32 v250, v168, v248, v247\n"
    "  v_fma_f32 v251, v169, v248, v247\n"
    "  v_add_f32 v246, v246, v252\n"
    "  v_cvt_pk_bf16_f32 v138, v252, v253\n"
    "  v_add_f32 v246, v246, v253\n"
    "  v_exp_f32 v254, v254\n"
    "  v_exp_f32 v255, v255\n"
    "  v_fma_f32 v252, v170, v248, v247\n"
    "  v_fma_f32 v253, v171, v248, v247\n"
    "  v_add_f32 v245, v245, v254\n"
    "  v_cvt_pk_bf16_f32 v139, v254, v255\n"
    "  v_add_f32 v245, v245, v255\n"
    "  v_exp_f32 v250, v250\n"
    "  v_exp_f32 v251, v251\n"
    "  v_fma_f32 v254, v172, v248, v247\n"
    "  v_fma_f32 v255, v173, v248, v247\n"
    "  v_add_f32 v246, v246, v250\n"
    "  v_cvt_pk_bf16_f32 v140, v250, v251\n"
    "  v_add_f32 v246, v246, v251\n"
    "  v_exp_f32 v252, v252\n"
    "  v_exp_f32 v253, v253\n"
    "  v_fma_f32 v250, v174, v248, v247\n"
    "  v_fma_f32 v251, v175, v248, v247\n"
    "  v_add_f32 v245, v245, v252\n"
    "  v_cvt_pk_bf16_f32 v141, v252, v253\n"
    "  v_add_f32 v245, v245, v253\n"
    "  v_exp_f32 v254, v254\n"
    "  v_exp_f32 v255, v255\n"
    "  v_fma_f32 v252, v176, v248, v247\n"
    "  v_fma_f32 v253, v177, v248, v247\n"
    "  v_add_f32 v246, v246, v254\n"
    "  v_cvt_pk_bf16_f32 v142, v254, v255\n"
    "  v_add_f32 v246, v246, v255\n"
    "  v_exp_f32 v250, v250\n"
    "  v_exp_f32 v251, v251\n"
    "  v_fma_f32 v254, v178, v248, v247\n"
    "  v_fma_f32 v255, v179, v248, v247\n"
    "  v_add_f32 v245, v245, v250\n"
    "  v_cvt_pk_bf16_f32 v143, v250, v251\n"
    "  v_add_f32 v245, v245, v251\n"
    "  v_exp_f32 v252, v252\n"
    "  v_exp_f32 v253, v253\n"
    "  v_fma_f32 v250, v180, v248, v247\n"
    "  v_fma_f32 v251, v181, v248, v247\n"
    "  v_add_f32 v246, v246, v252\n"
    "  v_cvt_pk_bf16_f32 v144, v252, v253\n"
    "  v_add_f32 v246, v246, v253\n"
    "  v_exp_f32 v254, v254\n"
    "  v_exp_f32 v255, v255\n"
    "  v_fma_f32 v252, v182, v248, v247\n"
    "  v_fma_f32 v253, v183, v248, v247\n"
    "  v_add_f32 v245, v245, v254\n"
    "  v_cvt_pk_bf16_f32 v145, v254, v255\n"
    "  v_add_f32 v245, v245, v255\n"
    "  v_exp_f32 v250, v250\n"
    "  v_exp_f32 v251, v251\n"
    "  v_fma_f32 v254, v184, v248, v247\n"
    "  v_fma_f32 v255, v185, v248, v247\n"
    "  v_add_f32 v246, v246, v250\n"
    "  v_cvt_pk_bf16_f32 v146, v250, v251\n"
    "  v_add_f32 v246, v246, v251\n"
    "  v_exp_f32 v252, v252\n"
    "  v_exp_f32 v253, v253\n"
    "  v_fma_f32 v250, v186, v248, v247\n"
    "  v_fma_f32 v251, v187, v248, v247\n"
    "  v_add_f32 v245, v245, v252\n"
    "  v_cvt_pk_bf16_f32 v147, v252, v253\n"
    "  v_add_f32 v245, v245, v253\n"
    "  v_exp_f32 v254, v254\n"
    "  v_exp_f32 v255, v255\n"
    "  v_fma_f32 v252, v188, v248, v247\n"
    "  v_fma_f32 v253, v189, v248, v247\n"
    "  v_add_f32 v246, v246, v254\n"
    "  v_cvt_pk_bf16_f32 v148, v254, v255\n"
    "  v_add_f32 v246, v246, v255\n"
    "  v_exp_f32 v250, v250\n"
    "  v_exp_f32 v251, v251\n"
    "  s_load_dwordx4 s[68:71], s[24:25], 0x0 glc\n"
    "  v_fma_f32 v254, v190, v248, v247\n"
    "  v_fma_f32 v255, v191, v248, v247\n"
    "  v_add_f32 v245, v245, v250\n"
    "  v_cvt_pk_bf16_f32 v149, v250, v251\n"
    "  v_add_f32 v245, v245, v251\n"
    "  v_exp_f32 v252, v252\n"
    "  v_exp_f32 v253, v253\n"
    "  v_fma_f32 v250, v192, v248, v247\n"
    "  v_fma_f32 v251, v193, v248, v247\n"
    "  v_add_f32 v246, v246, v252\n"
    "  v_cvt_pk_bf16_f32 v150, v252, v253\n"
    "  v_add_f32 v246, v246, v253\n"
    "  v_exp_f32 v254, v254\n"
    "  v_exp_f32 v255, v255\n"
    "  v_fma_f32 v252, v194, v248, v247\n"
    "  v_fma_f32 v253, v195, v248, v247\n"
    "  v_add_f32 v245, v245, v254\n"
    "  v_cvt_pk_bf16_f32 v151, v254, v255\n"
    "  v_add_f32 v245, v245, v255\n"
    "  v_exp_f32 v250, v250\n"
    "  v_exp_f32 v251, v251\n"
    "  v_fma_f32 v254, v196, v248, v247\n"
    "  v_fma_f32 v255, v197, v248, v247\n"
    "  v_add_f32 v246, v246, v250\n"
    "  v_cvt_pk_bf16_f32 v152, v250, v251\n"
    "  v_add_f32 v246, v246, v251\n"
    "  v_exp_f32 v252, v252\n"
    "  v_exp_f32 v253, v253\n"
    "  v_fma_f32 v250, v198, v248, v247\n"
    "  v_fma_f32 v251, v199, v248, v247\n"
    "  v_add_f32 v245, v245, v252\n"
    "  v_cvt_pk_bf16_f32 v153, v252, v253\n"
    "  v_add_f32 v245, v245, v253\n"
    "  v_exp_f32 v254, v254\n"
    "  v_exp_f32 v255, v255\n"
    "  v_fma_f32 v252, v200, v248, v247\n"
    "  v_fma_f32 v253, v201, v248, v247\n"
    "  v_add_f32 v246, v246, v254\n"
    "  v_cvt_pk_bf16_f32 v154, v254, v255\n"
    "  v_add_f32 v246, v246, v255\n"
    "  v_exp_f32 v250, v250\n"
    "  v_exp_f32 v251, v251\n"
    "  v_fma_f32 v254, v202, v248, v247\n"
    "  v_fma_f32 v255, v203, v248, v247\n"
    "  v_add_f32 v245, v245, v250\n"
    "  v_cvt_pk_bf16_f32 v155, v250, v251\n"
    "  v_add_f32 v245, v245, v251\n"
    "  v_exp_f32 v252, v252\n"
    "  v_exp_f32 v253, v253\n"
    "  v_fma_f32 v250, v204, v248, v247\n"
    "  v_fma_f32 v251, v205, v248, v247\n"
    "  v_add_f32 v246, v246, v252\n"
    "  v_cvt_pk_bf16_f32 v156, v252, v253\n"
    "  v_add_f32 v246, v246, v253\n"
    "  v_exp_f32 v254, v254\n"
    "  v_exp_f32 v255, v255\n"
    "  v_fma_f32 v252, v206, v248, v247\n"
    "  v_fma_f32 v253, v207, v248, v247\n"
    "  v_add_f32 v245, v245, v254\n"
    "  v_cvt_pk_bf16_f32 v157, v254, v255\n"
    "  v_add_f32 v245, v245, v255\n"
    "  v_exp_f32 v250, v250\n"
    "  v_exp_f32 v251, v251\n"
    "  v_add_f32 v246, v246, v250\n"
    "  v_cvt_pk_bf16_f32 v158, v250, v251\n"
    "  v_add_f32 v246, v246, v251\n"
    "  v_exp_f32 v252, v252\n"
    "  v_exp_f32 v253, v253\n"
    "  v_add_f32 v245, v245, v252\n"
    "  v_cvt_pk_bf16_f32 v159, v252, v253\n"
    "  v_add_f32 v245, v245, v253\n"
    "  v_add_f32 v245, v245, v246\n"
    "  v_mov_b32 v250, v245\n"
    "  s_nop 1\n"
    "  v_permlane32_swap_b32 v250, v245\n"
    "  v_add_f32 v245, v245, v250\n"
    "  v_rcp_f32 v249, v245\n"
    "  v_add_u32 v9, s27, v10\n"
    "  s_setprio 2\n"
    "  s_cmp_gt_u32 s26, 3\n"
    "  s_cbranch_scc1 .Lgqa_prl\n"
    "  s_setprio 3\n"
    ".Lgqa_prl:\n"
    "  s_mov_b32 s67, 0\n"
    ".Lgqa_fv0:\n"
    "  s_waitcnt lgkmcnt(0)\n"
    "  s_min_u32 s68, s68, s69\n"
    "  s_min_u32 s68, s68, s70\n"
    "  s_min_u32 s68, s68, s71\n"
    "  s_cmp_ge_u32 s68, 1\n"
    "  s_cbranch_scc1 .Lgqa_fv0_ok\n"
    "  s_add_u32 s67, s67, 1\n"
    "  s_cmp_gt_u32 s67, 0x8000\n"
    "  s_cbranch_scc1 .Lgqa_fv0_ok\n"
    "  s_load_dwordx4 s[68:71], s[24:25], 0x0 glc\n"
    "  s_branch .Lgqa_fv0\n"
    ".Lgqa_fv0_ok:\n"
    "  s_cmp_ge_u32 s68, 2\n"
    "  s_cselect_b32 s76, 1, 0\n"
    "  ds_read_b64_tr_b16 v[224:225], v5 offset:0\n"
    "  ds_read_b64_tr_b16 v[226:227], v5 offset:2048\n"
    "  ds_read_b64_tr_b16 v[228:229], v6 offset:0\n"
    "  ds_read_b64_tr_b16 v[230:231], v6 offset:2048\n"
    "  ds_read_b64_tr_b16 v[232:233], v7 offset:0\n"
    "  ds_read_b64_tr_b16 v[234:235], v7 offset:2048\n"
    "  ds_read_b64_tr_b16 v[236:237], v8 offset:0\n"
    "  ds_read_b64_tr_b16 v[238:239], v8 offset:2048\n"
    "  s_waitcnt lgkmcnt(4)\n"
    "  v_mfma_f32_32x32x16_bf16 v[16:31], v[224:227], v[208:211], 0\n"
    "  ds_read_b64_tr_b16 v[240:241], v5 offset:4096\n"
    "  ds_read_b64_tr_b16 v[242:243], v5 offset:6144\n"
    "  v_mfma_f32_32x32x16_bf16 v[32:47], v[228:231], v[208:211], 0\n"
    "  ds_read_b64_tr_b16 v[224:225], v6 offset:4096\n"
    "  ds_read_b64_tr_b16 v[226:227], v6 offset:6144\n"
    "  s_waitcnt lgkmcnt(4)\n"
    "  v_mfma_f32_32x32x16_bf16 v[80:95], v[232:235], v[208:211], 0\n"
    "  ds_read_b64_tr_b16 v[228:229], v7 offset:4096\n"
    "  ds_read_b64_tr_b16 v[230:231], v7 offset:6144\n"
    "  v_mfma_f32_32x32x16_bf16 v[96:111], v[236:239], v[208:211], 0\n"
    "  ds_read_b64_tr_b16 v[232:233], v8 offset:4096\n"
    "  ds_read_b64_tr_b16 v[234:235], v8 offset:6144\n"
    "  s_waitcnt lgkmcnt(4)\n"
    "  v_mfma_f32_32x32x16_bf16 v[16:31], v[240:243], v[212:215], v[16:31]\n"
    "  ds_read_b64_tr_b16 v[236:237], v5 offset:8192\n"
    "  ds_read_b64_tr_b16 v[238:239], v5 offset:10240\n"
    "  v_mfma_f32_32x32x16_bf16 v[32:47], v[224:227], v[212:215], v[32:47]\n"
    "  ds_read_b64_tr_b16 v[240:241], v6 offset:8192\n"
    "  ds_read_b64_tr_b16 v[242:243], v6 offset:10240\n"
    "  s_waitcnt lgkmcnt(4)\n"
    "  v_mfma_f32_32x32x16_bf16 v[80:95], v[228:231], v[212:215], v[80:95]\n"
    "  ds_read_b64_tr_b16 v[224:225], v7 offset:8192\n"
    "  ds_read_b64_tr_b16 v[226:227], v7 offset:10240\n"
    "  v_mfma_f32_32x32x16_bf16 v[96:111], v[232:235], v[212:215], v[96:111]\n"
    "  ds_read_b64_tr_b16 v[228:229], v8 offset:8192\n"
    "  ds_read_b64_tr_b16 v[230:231], v8 offset:10240\n"
    "  s_waitcnt lgkmcnt(4)\n"
    "  v_mfma_f32_32x32x16_bf16 v[16:31], v[236:239], v[48:51], v[16:31]\n"
    "  ds_read_b64_tr_b16 v[232:233], v5 offset:12288\n"
    "  ds_read_b64_tr_b16 v[234:235], v5 offset:14336\n"
    "  v_mfma_f32_32x32x16_bf16 v[32:47], v[240:243], v[48:51], v[32:47]\n"
    "  ds_read_b64_tr_b16 v[236:237], v6 offset:12288\n"
    "  ds_read_b64_tr_b16 v[238:239], v6 offset:14336\n"
    "  s_waitcnt lgkmcnt(4)\n"
    "  v_mfma_f32_32x32x16_bf16 v[80:95], v[224:227], v[48:51], v[80:95]\n"
    "  ds_read_b64_tr_b16 v[240:241], v7 offset:12288\n"
    "  ds_read_b64_tr_b16 v[242:243], v7 offset:14336\n"
    "  v_mfma_f32_32x32x16_bf16 v[96:111], v[228:231], v[48:51], v[96:111]\n"
    "  ds_read_b64_tr_b16 v[224:225], v8 offset:12288\n"
    "  ds_read_b64_tr_b16 v[226:227], v8 offset:14336\n"
    "  s_waitcnt lgkmcnt(4)\n"
    "  v_mfma_f32_32x32x16_bf16 v[16:31], v[232:235], v[52:55], v[16:31]\n"
    "  ds_read_b64_tr_b16 v[228:229], v5 offset:16384\n"
    "  ds_read_b64_tr_b16 v[230:231], v5 offset:18432\n"
    "  v_mfma_f32_32x32x16_bf16 v[32:47], v[236:239], v[52:55], v[32:47]\n"
    "  ds_read_b64_tr_b16 v[232:233], v6 offset:16384\n"
    "  ds_read_b64_tr_b16 v[234:235], v6 offset:18432\n"
    "  s_waitcnt lgkmcnt(4)\n"
    "  v_mfma_f32_32x32x16_bf16 v[80:95], v[240:243], v[52:55], v[80:95]\n"
    "  ds_read_b64_tr_b16 v[236:237], v7 offset:16384\n"
    "  ds_read_b64_tr_b16 v[238:239], v7 offset:18432\n"
    "  v_mfma_f32_32x32x16_bf16 v[96:111], v[224:227], v[52:55], v[96:111]\n"
    "  ds_read_b64_tr_b16 v[240:241], v8 offset:16384\n"
    "  ds_read_b64_tr_b16 v[242:243], v8 offset:18432\n"
    "  s_waitcnt lgkmcnt(4)\n"
    "  v_mfma_f32_32x32x16_bf16 v[16:31], v[228:231], v[56:59], v[16:31]\n"
    "  ds_read_b64_tr_b16 v[224:225], v5 offset:20480\n"
    "  ds_read_b64_tr_b16 v[226:227], v5 offset:22528\n"
    "  v_and_b32 v176, 0x7c, v13\n"
    "  v_lshrrev_b32 v177, 7, v13\n"
    "  v_or_b32 v176, v176, v177\n"
    "  v_lshlrev_b32 v176, 6, v176\n"
    "  s_cmp_gt_u32 s26, 3\n"
    "  s_cbranch_scc1 .Lgqa_skq\n"
    "  buffer_load_dwordx4 v[112:115], v176, s[4:7], s28 offen offset:0 nt\n"
    "  buffer_load_dwordx4 v[116:119], v176, s[4:7], s28 offen offset:16 nt\n"
    "  buffer_load_dwordx4 v[120:123], v176, s[4:7], s28 offen offset:32 nt\n"
    "  buffer_load_dwordx4 v[124:127], v176, s[4:7], s28 offen offset:48 nt\n"
    "  buffer_load_dwordx4 v[208:211], v176, s[4:7], s28 offen offset:128 nt\n"
    "  buffer_load_dwordx4 v[212:215], v176, s[4:7], s28 offen offset:144 nt\n"
    "  buffer_load_dwordx4 v[48:51], v176, s[4:7], s28 offen offset:160 nt\n"
    "  buffer_load_dwordx4 v[52:55], v176, s[4:7], s28 offen offset:176 nt\n"
    ".Lgqa_skq:\n"
    "  v_mfma_f32_32x32x16_bf16 v[32:47], v[232:235], v[56:59], v[32:47]\n"
    "  ds_read_b64_tr_b16 v[228:229], v6 offset:20480\n"
    "  ds_read_b64_tr_b16 v[230:231], v6 offset:22528\n"
    "  s_waitcnt lgkmcnt(4)\n"
    "  v_mfma_f32_32x32x16_bf16 v[80:95], v[236:239], v[56:59], v[80:95]\n"
    "  ds_read_b64_tr_b16 v[232:233], v7 offset:20480\n"
    "  ds_read_b64_tr_b16 v[234:235], v7 offset:22528\n"
    "  v_mfma_f32_32x32x16_bf16 v[96:111], v[240:243], v[56:59], v[96:111]\n"
    "  ds_read_b64_tr_b16 v[236:237], v8 offset:20480\n"
    "  ds_read_b64_tr_b16 v[238:239], v8 offset:22528\n"
    "  s_waitcnt lgkmcnt(4)\n"
    "  v_mfma_f32_32x32x16_bf16 v[16:31], v[224:227], v[60:63], v[16:31]\n"
    "  ds_read_b64_tr_b16 v[240:241], v5 offset:24576\n"
    "  ds_read_b64_tr_b16 v[242:243], v5 offset:26624\n"
    "  v_mfma_f32_32x32x16_bf16 v[32:47], v[228:231], v[60:63], v[32:47]\n"
    "  ds_read_b64_tr_b16 v[224:225], v6 offset:24576\n"
    "  ds_read_b64_tr_b16 v[226:227], v6 offset:26624\n"
    "  s_waitcnt lgkmcnt(4)\n"
    "  v_mfma_f32_32x32x16_bf16 v[80:95], v[232:235], v[60:63], v[80:95]\n"
    "  ds_read_b64_tr_b16 v[228:229], v7 offset:24576\n"
    "  ds_read_b64_tr_b16 v[230:231], v7 offset:26624\n"
    "  v_mfma_f32_32x32x16_bf16 v[96:111], v[236:239], v[60:63], v[96:111]\n"
    "  ds_read_b64_tr_b16 v[232:233], v8 offset:24576\n"
    "  ds_read_b64_tr_b16 v[234:235], v8 offset:26624\n"
    "  s_waitcnt lgkmcnt(4)\n"
    "  v_mfma_f32_32x32x16_bf16 v[16:31], v[240:243], v[64:67], v[16:31]\n"
    "  ds_read_b64_tr_b16 v[236:237], v5 offset:28672\n"
    "  ds_read_b64_tr_b16 v[238:239], v5 offset:30720\n"
    "  v_mfma_f32_32x32x16_bf16 v[32:47], v[224:227], v[64:67], v[32:47]\n"
    "  ds_read_b64_tr_b16 v[240:241], v6 offset:28672\n"
    "  ds_read_b64_tr_b16 v[242:243], v6 offset:30720\n"
    "  s_waitcnt lgkmcnt(4)\n"
    "  v_mfma_f32_32x32x16_bf16 v[80:95], v[228:231], v[64:67], v[80:95]\n"
    "  ds_read_b64_tr_b16 v[224:225], v7 offset:28672\n"
    "  ds_read_b64_tr_b16 v[226:227], v7 offset:30720\n"
    "  v_mfma_f32_32x32x16_bf16 v[96:111], v[232:235], v[64:67], v[96:111]\n"
    "  ds_read_b64_tr_b16 v[228:229], v8 offset:28672\n"
    "  ds_read_b64_tr_b16 v[230:231], v8 offset:30720\n"
    "  s_waitcnt lgkmcnt(4)\n"
    "  v_mfma_f32_32x32x16_bf16 v[16:31], v[236:239], v[68:71], v[16:31]\n"
    "  ds_read_b64_tr_b16 v[232:233], v5 offset:32768\n"
    "  ds_read_b64_tr_b16 v[234:235], v5 offset:34816\n"
    "  v_mfma_f32_32x32x16_bf16 v[32:47], v[240:243], v[68:71], v[32:47]\n"
    "  ds_read_b64_tr_b16 v[236:237], v6 offset:32768\n"
    "  ds_read_b64_tr_b16 v[238:239], v6 offset:34816\n"
    "  s_waitcnt lgkmcnt(4)\n"
    "  v_mfma_f32_32x32x16_bf16 v[80:95], v[224:227], v[68:71], v[80:95]\n"
    "  ds_read_b64_tr_b16 v[240:241], v7 offset:32768\n"
    "  ds_read_b64_tr_b16 v[242:243], v7 offset:34816\n"
    "  v_mfma_f32_32x32x16_bf16 v[96:111], v[228:231], v[68:71], v[96:111]\n"
    "  ds_read_b64_tr_b16 v[224:225], v8 offset:32768\n"
    "  ds_read_b64_tr_b16 v[226:227], v8 offset:34816\n"
    "  s_waitcnt lgkmcnt(4)\n"
    "  v_mfma_f32_32x32x16_bf16 v[16:31], v[232:235], v[72:75], v[16:31]\n"
    "  ds_read_b64_tr_b16 v[228:229], v5 offset:36864\n"
    "  ds_read_b64_tr_b16 v[230:231], v5 offset:38912\n"
    "  v_mfma_f32_32x32x16_bf16 v[32:47], v[236:239], v[72:75], v[32:47]\n"
    "  ds_read_b64_tr_b16 v[232:233], v6 offset:36864\n"
    "  ds_read_b64_tr_b16 v[234:235], v6 offset:38912\n"
    "  s_waitcnt lgkmcnt(4)\n"
    "  v_mfma_f32_32x32x16_bf16 v[80:95], v[240:243], v[72:75], v[80:95]\n"
    "  ds_read_b64_tr_b16 v[236:237], v7 offset:36864\n"
    "  ds_read_b64_tr_b16 v[238:239], v7 offset:38912\n"
    "  v_mfma_f32_32x32x16_bf16 v[96:111], v[224:227], v[72:75], v[96:111]\n"
    "  ds_read_b64_tr_b16 v[240:241], v8 offset:36864\n"
    "  ds_read_b64_tr_b16 v[242:243], v8 offset:38912\n"
    "  s_cmp_lg_u32 s76, 0\n"
    "  s_cbranch_scc1 .Lgqa_fv1_ok\n"
    "  s_load_dwordx4 s[68:71], s[24:25], 0x0 glc\n"
    "  s_mov_b32 s67, 0\n"
    ".Lgqa_fv1:\n"
    "  s_waitcnt lgkmcnt(0)\n"
    "  s_min_u32 s68, s68, s69\n"
    "  s_min_u32 s68, s68, s70\n"
    "  s_min_u32 s68, s68, s71\n"
    "  s_cmp_ge_u32 s68, 2\n"
    "  s_cbranch_scc1 .Lgqa_fv1_ok\n"
    "  s_add_u32 s67, s67, 1\n"
    "  s_cmp_gt_u32 s67, 0x8000\n"
    "  s_cbranch_scc1 .Lgqa_fv1_ok\n"
    "  s_load_dwordx4 s[68:71], s[24:25], 0x0 glc\n"
    "  s_branch .Lgqa_fv1\n"
    ".Lgqa_fv1_ok:\n"
    "  s_waitcnt lgkmcnt(4)\n"
    "  v_mfma_f32_32x32x16_bf16 v[16:31], v[228:231], v[76:79], v[16:31]\n"
    "  v_add_u32 v5, 0xa000, v5\n"
    "  v_add_u32 v6, 0xa000, v6\n"
    "  v_add_u32 v7, 0xa000, v7\n"
    "  v_add_u32 v8, 0xa000, v8\n"
    "  ds_read_b64_tr_b16 v[224:225], v5 offset:0\n"
    "  ds_read_b64_tr_b16 v[226:227], v5 offset:2048\n"
    "  v_mfma_f32_32x32x16_bf16 v[32:47], v[232:235], v[76:79], v[32:47]\n"
    "  ds_read_b64_tr_b16 v[228:229], v5 offset:4096\n"
    "  ds_read_b64_tr_b16 v[230:231], v5 offset:6144\n"
    "  s_waitcnt lgkmcnt(4)\n"
    "  v_mfma_f32_32x32x16_bf16 v[80:95], v[236:239], v[76:79], v[80:95]\n"
    "  ds_read_b64_tr_b16 v[232:233], v5 offset:8192\n"
    "  ds_read_b64_tr_b16 v[234:235], v5 offset:10240\n"
    "  v_mfma_f32_32x32x16_bf16 v[96:111], v[240:243], v[76:79], v[96:111]\n"
    "  ds_read_b64_tr_b16 v[236:237], v5 offset:12288\n"
    "  ds_read_b64_tr_b16 v[238:239], v5 offset:14336\n"
    "  s_waitcnt lgkmcnt(4)\n"
    "  v_mfma_f32_32x32x16_bf16 v[16:31], v[224:227], v[216:219], v[16:31]\n"
    "  ds_read_b64_tr_b16 v[240:241], v5 offset:16384\n"
    "  ds_read_b64_tr_b16 v[242:243], v5 offset:18432\n"
    "  v_mfma_f32_32x32x16_bf16 v[16:31], v[228:231], v[220:223], v[16:31]\n"
    "  ds_read_b64_tr_b16 v[224:225], v5 offset:20480\n"
    "  ds_read_b64_tr_b16 v[226:227], v5 offset:22528\n"
    "  s_waitcnt lgkmcnt(4)\n"
    "  v_mfma_f32_32x32x16_bf16 v[16:31], v[232:235], v[128:131], v[16:31]\n"
    "  ds_read_b64_tr_b16 v[228:229], v5 offset:24576\n"
    "  ds_read_b64_tr_b16 v[230:231], v5 offset:26624\n"
    "  v_mfma_f32_32x32x16_bf16 v[16:31], v[236:239], v[132:135], v[16:31]\n"
    "  ds_read_b64_tr_b16 v[232:233], v5 offset:28672\n"
    "  ds_read_b64_tr_b16 v[234:235], v5 offset:30720\n"
    "  s_waitcnt lgkmcnt(4)\n"
    "  v_mfma_f32_32x32x16_bf16 v[16:31], v[240:243], v[136:139], v[16:31]\n"
    "  ds_read_b64_tr_b16 v[236:237], v5 offset:32768\n"
    "  ds_read_b64_tr_b16 v[238:239], v5 offset:34816\n"
    "  v_mfma_f32_32x32x16_bf16 v[16:31], v[224:227], v[140:143], v[16:31]\n"
    "  ds_read_b64_tr_b16 v[240:241], v5 offset:36864\n"
    "  ds_read_b64_tr_b16 v[242:243], v5 offset:38912\n"
    "  s_waitcnt lgkmcnt(4)\n"
    "  v_mfma_f32_32x32x16_bf16 v[16:31], v[228:231], v[144:147], v[16:31]\n"
    "  ds_read_b64_tr_b16 v[224:225], v6 offset:0\n"
    "  ds_read_b64_tr_b16 v[226:227], v6 offset:2048\n"
    "  v_mfma_f32_32x32x16_bf16 v[16:31], v[232:235], v[148:151], v[16:31]\n"
    "  ds_read_b64_tr_b16 v[228:229], v6 offset:4096\n"
    "  ds_read_b64_tr_b16 v[230:231], v6 offset:6144\n"
    "  s_waitcnt lgkmcnt(4)\n"
    "  v_mfma_f32_32x32x16_bf16 v[16:31], v[236:239], v[152:155], v[16:31]\n"
    "  ds_read_b64_tr_b16 v[232:233], v6 offset:8192\n"
    "  ds_read_b64_tr_b16 v[234:235], v6 offset:10240\n"
    "  v_mfma_f32_32x32x16_bf16 v[16:31], v[240:243], v[156:159], v[16:31]\n"
    "  ds_read_b64_tr_b16 v[236:237], v6 offset:12288\n"
    "  ds_read_b64_tr_b16 v[238:239], v6 offset:14336\n"
    "  s_waitcnt lgkmcnt(4)\n"
    "  v_mfma_f32_32x32x16_bf16 v[32:47], v[224:227], v[216:219], v[32:47]\n"
    "  ds_read_b64_tr_b16 v[240:241], v6 offset:16384\n"
    "  ds_read_b64_tr_b16 v[242:243], v6 offset:18432\n"
    "  v_mfma_f32_32x32x16_bf16 v[32:47], v[228:231], v[220:223], v[32:47]\n"
    "  ds_read_b64_tr_b16 v[224:225], v6 offset:20480\n"
    "  ds_read_b64_tr_b16 v[226:227], v6 offset:22528\n"
    "  s_waitcnt lgkmcnt(4)\n"
    "  v_mfma_f32_32x32x16_bf16 v[32:47], v[232:235], v[128:131], v[32:47]\n"
    "  ds_read_b64_tr_b16 v[228:229], v6 offset:24576\n"
    "  ds_read_b64_tr_b16 v[230:231], v6 offset:26624\n"
    "  v_mfma_f32_32x32x16_bf16 v[32:47], v[236:239], v[132:135], v[32:47]\n"
    "  ds_read_b64_tr_b16 v[232:233], v6 offset:28672\n"
    "  ds_read_b64_tr_b16 v[234:235], v6 offset:30720\n"
    "  s_waitcnt lgkmcnt(4)\n"
    "  v_mfma_f32_32x32x16_bf16 v[32:47], v[240:243], v[136:139], v[32:47]\n"
    "  ds_read_b64_tr_b16 v[236:237], v6 offset:32768\n"
    "  ds_read_b64_tr_b16 v[238:239], v6 offset:34816\n"
    "  v_mfma_f32_32x32x16_bf16 v[32:47], v[224:227], v[140:143], v[32:47]\n"
    "  ds_read_b64_tr_b16 v[240:241], v6 offset:36864\n"
    "  ds_read_b64_tr_b16 v[242:243], v6 offset:38912\n"
    "  s_waitcnt lgkmcnt(4)\n"
    "  v_mfma_f32_32x32x16_bf16 v[32:47], v[228:231], v[144:147], v[32:47]\n"
    "  ds_read_b64_tr_b16 v[224:225], v7 offset:0\n"
    "  ds_read_b64_tr_b16 v[226:227], v7 offset:2048\n"
    "  v_mfma_f32_32x32x16_bf16 v[32:47], v[232:235], v[148:151], v[32:47]\n"
    "  ds_read_b64_tr_b16 v[228:229], v7 offset:4096\n"
    "  ds_read_b64_tr_b16 v[230:231], v7 offset:6144\n"
    "  s_waitcnt lgkmcnt(4)\n"
    "  v_mfma_f32_32x32x16_bf16 v[32:47], v[236:239], v[152:155], v[32:47]\n"
    "  ds_read_b64_tr_b16 v[232:233], v7 offset:8192\n"
    "  ds_read_b64_tr_b16 v[234:235], v7 offset:10240\n"
    "  v_mfma_f32_32x32x16_bf16 v[32:47], v[240:243], v[156:159], v[32:47]\n"
    "  ds_read_b64_tr_b16 v[236:237], v7 offset:12288\n"
    "  ds_read_b64_tr_b16 v[238:239], v7 offset:14336\n"
    "  s_waitcnt lgkmcnt(4)\n"
    "  v_mfma_f32_32x32x16_bf16 v[80:95], v[224:227], v[216:219], v[80:95]\n"
    "  ds_read_b64_tr_b16 v[240:241], v7 offset:16384\n"
    "  ds_read_b64_tr_b16 v[242:243], v7 offset:18432\n"
    "  v_mfma_f32_32x32x16_bf16 v[80:95], v[228:231], v[220:223], v[80:95]\n"
    "  ds_read_b64_tr_b16 v[224:225], v7 offset:20480\n"
    "  ds_read_b64_tr_b16 v[226:227], v7 offset:22528\n"
    "  v_mul_f32 v16, v16, v249\n"
    "  v_mul_f32 v17, v17, v249\n"
    "  v_cvt_pk_bf16_f32 v160, v16, v17\n"
    "  v_mul_f32 v24, v24, v249\n"
    "  v_mul_f32 v25, v25, v249\n"
    "  s_waitcnt lgkmcnt(4)\n"
    "  v_mfma_f32_32x32x16_bf16 v[80:95], v[232:235], v[128:131], v[80:95]\n"
    "  ds_read_b64_tr_b16 v[228:229], v7 offset:24576\n"
    "  ds_read_b64_tr_b16 v[230:231], v7 offset:26624\n"
    "  v_cvt_pk_bf16_f32 v164, v24, v25\n"
    "  v_mul_f32 v32, v32, v249\n"
    "  v_mul_f32 v33, v33, v249\n"
    "  v_cvt_pk_bf16_f32 v168, v32, v33\n"
    "  v_mul_f32 v40, v40, v249\n"
    "  v_mfma_f32_32x32x16_bf16 v[80:95], v[236:239], v[132:135], v[80:95]\n"
    "  ds_read_b64_tr_b16 v[232:233], v7 offset:28672\n"
    "  ds_read_b64_tr_b16 v[234:235], v7 offset:30720\n"
    "  v_mul_f32 v41, v41, v249\n"
    "  v_cvt_pk_bf16_f32 v172, v40, v41\n"
    "  v_mul_f32 v18, v18, v249\n"
    "  v_mul_f32 v19, v19, v249\n"
    "  v_cvt_pk_bf16_f32 v161, v18, v19\n"
    "  s_waitcnt lgkmcnt(4)\n"
    "  v_mfma_f32_32x32x16_bf16 v[80:95], v[240:243], v[136:139], v[80:95]\n"
    "  ds_read_b64_tr_b16 v[236:237], v7 offset:32768\n"
    "  ds_read_b64_tr_b16 v[238:239], v7 offset:34816\n"
    "  v_mul_f32 v26, v26, v249\n"
    "  v_mul_f32 v27, v27, v249\n"
    "  v_cvt_pk_bf16_f32 v165, v26, v27\n"
    "  v_mul_f32 v34, v34, v249\n"
    "  v_mfma_f32_32x32x16_bf16 v[80:95], v[224:227], v[140:143], v[80:95]\n"
    "  ds_read_b64_tr_b16 v[240:241], v7 offset:36864\n"
    "  ds_read_b64_tr_b16 v[242:243], v7 offset:38912\n"
    "  v_mul_f32 v35, v35, v249\n"
    "  v_cvt_pk_bf16_f32 v169, v34, v35\n"
    "  v_mul_f32 v42, v42, v249\n"
    "  v_mul_f32 v43, v43, v249\n"
    "  v_cvt_pk_bf16_f32 v173, v42, v43\n"
    "  s_waitcnt lgkmcnt(4)\n"
    "  v_mfma_f32_32x32x16_bf16 v[80:95], v[228:231], v[144:147], v[80:95]\n"
    "  ds_read_b64_tr_b16 v[224:225], v8 offset:0\n"
    "  ds_read_b64_tr_b16 v[226:227], v8 offset:2048\n"
    "  s_mov_b64 vcc, s[58:59]\n"
    "  v_mul_f32 v20, v20, v249\n"
    "  v_mul_f32 v21, v21, v249\n"
    "  v_cvt_pk_bf16_f32 v162, v20, v21\n"
    "  v_cndmask_b32_dpp v176, v164, v160, vcc quad_perm:[1,0,3,2] row_mask:0xf bank_mask:0xf\n"
    "  v_mfma_f32_32x32x16_bf16 v[80:95], v[232:235], v[148:151], v[80:95]\n"
    "  ds_read_b64_tr_b16 v[228:229], v8 offset:4096\n"
    "  ds_read_b64_tr_b16 v[230:231], v8 offset:6144\n"
    "  v_mul_f32 v28, v28, v249\n"
    "  v_mul_f32 v29, v29, v249\n"
    "  v_cvt_pk_bf16_f32 v166, v28, v29\n"
    "  v_cndmask_b32_dpp v177, v165, v161, vcc quad_perm:[1,0,3,2] row_mask:0xf bank_mask:0xf\n"
    "  s_waitcnt lgkmcnt(4)\n"
    "  v_mfma_f32_32x32x16_bf16 v[80:95], v[236:239], v[152:155], v[80:95]\n"
    "  ds_read_b64_tr_b16 v[232:233], v8 offset:8192\n"
    "  ds_read_b64_tr_b16 v[234:235], v8 offset:10240\n"
    "  v_mul_f32 v36, v36, v249\n"
    "  v_mul_f32 v37, v37, v249\n"
    "  v_cvt_pk_bf16_f32 v170, v36, v37\n"
    "  v_cndmask_b32_dpp v184, v172, v168, vcc quad_perm:[1,0,3,2] row_mask:0xf bank_mask:0xf\n"
    "  v_mul_f32 v44, v44, v249\n"
    "  v_mfma_f32_32x32x16_bf16 v[80:95], v[240:243], v[156:159], v[80:95]\n"
    "  ds_read_b64_tr_b16 v[236:237], v8 offset:12288\n"
    "  ds_read_b64_tr_b16 v[238:239], v8 offset:14336\n"
    "  v_mul_f32 v45, v45, v249\n"
    "  v_cvt_pk_bf16_f32 v174, v44, v45\n"
    "  v_cndmask_b32_dpp v185, v173, v169, vcc quad_perm:[1,0,3,2] row_mask:0xf bank_mask:0xf\n"
    "  v_mul_f32 v22, v22, v249\n"
    "  v_mul_f32 v23, v23, v249\n"
    "  s_waitcnt lgkmcnt(4)\n"
    "  v_mfma_f32_32x32x16_bf16 v[96:111], v[224:227], v[216:219], v[96:111]\n"
    "  ds_read_b64_tr_b16 v[240:241], v8 offset:16384\n"
    "  ds_read_b64_tr_b16 v[242:243], v8 offset:18432\n"
    "  v_cvt_pk_bf16_f32 v163, v22, v23\n"
    "  s_mov_b64 vcc, s[60:61]\n"
    "  v_mul_f32 v30, v30, v249\n"
    "  v_mul_f32 v31, v31, v249\n"
    "  v_mfma_f32_32x32x16_bf16 v[96:111], v[228:231], v[220:223], v[96:111]\n"
    "  ds_read_b64_tr_b16 v[224:225], v8 offset:20480\n"
    "  ds_read_b64_tr_b16 v[226:227], v8 offset:22528\n"
    "  v_cvt_pk_bf16_f32 v167, v30, v31\n"
    "  v_cndmask_b32_dpp v180, v160, v164, vcc quad_perm:[1,0,3,2] row_mask:0xf bank_mask:0xf\n"
    "  v_mul_f32 v38, v38, v249\n"
    "  v_mul_f32 v39, v39, v249\n"
    "  v_cvt_pk_bf16_f32 v171, v38, v39\n"
    "  s_waitcnt lgkmcnt(4)\n"
    "  v_mfma_f32_32x32x16_bf16 v[96:111], v[232:235], v[128:131], v[96:111]\n"
    "  ds_read_b64_tr_b16 v[228:229], v8 offset:24576\n"
    "  ds_read_b64_tr_b16 v[230:231], v8 offset:26624\n"
    "  v_cndmask_b32_dpp v181, v161, v165, vcc quad_perm:[1,0,3,2] row_mask:0xf bank_mask:0xf\n"
    "  v_mul_f32 v46, v46, v249\n"
    "  v_mul_f32 v47, v47, v249\n"
    "  v_cvt_pk_bf16_f32 v175, v46, v47\n"
    "  v_cndmask_b32_dpp v188, v168, v172, vcc quad_perm:[1,0,3,2] row_mask:0xf bank_mask:0xf\n"
    "  v_mfma_f32_32x32x16_bf16 v[96:111], v[236:239], v[132:135], v[96:111]\n"
    "  ds_read_b64_tr_b16 v[232:233], v8 offset:28672\n"
    "  ds_read_b64_tr_b16 v[234:235], v8 offset:30720\n"
    "  v_cndmask_b32_dpp v189, v169, v173, vcc quad_perm:[1,0,3,2] row_mask:0xf bank_mask:0xf\n"
    "  v_mov_b32 v255, v255\n"
    "  v_cndmask_b32_dpp v182, v162, v166, vcc quad_perm:[1,0,3,2] row_mask:0xf bank_mask:0xf\n"
    "  v_mov_b32 v255, v255\n"
    "  v_cndmask_b32_dpp v183, v163, v167, vcc quad_perm:[1,0,3,2] row_mask:0xf bank_mask:0xf\n"
    "  v_mov_b32 v255, v255\n"
    "  v_cndmask_b32_dpp v190, v170, v174, vcc quad_perm:[1,0,3,2] row_mask:0xf bank_mask:0xf\n"
    "  s_waitcnt lgkmcnt(4)\n"
    "  v_mfma_f32_32x32x16_bf16 v[96:111], v[240:243], v[136:139], v[96:111]\n"
    "  ds_read_b64_tr_b16 v[236:237], v8 offset:32768\n"
    "  ds_read_b64_tr_b16 v[238:239], v8 offset:34816\n"
    "  v_cndmask_b32_dpp v191, v171, v175, vcc quad_perm:[1,0,3,2] row_mask:0xf bank_mask:0xf\n"
    "  s_mov_b64 vcc, s[58:59]\n"
    "  v_cndmask_b32_dpp v178, v166, v162, vcc quad_perm:[1,0,3,2] row_mask:0xf bank_mask:0xf\n"
    "  v_mov_b32 v255, v255\n"
    "  v_cndmask_b32_dpp v179, v167, v163, vcc quad_perm:[1,0,3,2] row_mask:0xf bank_mask:0xf\n"
    "  v_mov_b32 v255, v255\n"
    "  v_cndmask_b32_dpp v186, v174, v170, vcc quad_perm:[1,0,3,2] row_mask:0xf bank_mask:0xf\n"
    "  v_mfma_f32_32x32x16_bf16 v[96:111], v[224:227], v[140:143], v[96:111]\n"
    "  ds_read_b64_tr_b16 v[240:241], v8 offset:36864\n"
    "  ds_read_b64_tr_b16 v[242:243], v8 offset:38912\n"
    "  v_cndmask_b32_dpp v187, v175, v171, vcc quad_perm:[1,0,3,2] row_mask:0xf bank_mask:0xf\n"
    "  s_mov_b64 vcc, s[62:63]\n"
    "  v_cndmask_b32_dpp v160, v184, v176, vcc quad_perm:[2,3,0,1] row_mask:0xf bank_mask:0xf\n"
    "  v_mov_b32 v255, v255\n"
    "  v_cndmask_b32_dpp v161, v185, v177, vcc quad_perm:[2,3,0,1] row_mask:0xf bank_mask:0xf\n"
    "  v_mov_b32 v255, v255\n"
    "  v_cndmask_b32_dpp v162, v186, v178, vcc quad_perm:[2,3,0,1] row_mask:0xf bank_mask:0xf\n"
    "  s_waitcnt lgkmcnt(4)\n"
    "  v_mfma_f32_32x32x16_bf16 v[96:111], v[228:231], v[144:147], v[96:111]\n"
    "  v_cndmask_b32_dpp v163, v187, v179, vcc quad_perm:[2,3,0,1] row_mask:0xf bank_mask:0xf\n"
    "  v_mov_b32 v255, v255\n"
    "  v_cndmask_b32_dpp v164, v188, v180, vcc quad_perm:[2,3,0,1] row_mask:0xf bank_mask:0xf\n"
    "  v_mov_b32 v255, v255\n"
    "  v_cndmask_b32_dpp v165, v189, v181, vcc quad_perm:[2,3,0,1] row_mask:0xf bank_mask:0xf\n"
    "  v_mov_b32 v255, v255\n"
    "  v_cndmask_b32_dpp v166, v190, v182, vcc quad_perm:[2,3,0,1] row_mask:0xf bank_mask:0xf\n"
    "  v_mfma_f32_32x32x16_bf16 v[96:111], v[232:235], v[148:151], v[96:111]\n"
    "  v_cndmask_b32_dpp v167, v191, v183, vcc quad_perm:[2,3,0,1] row_mask:0xf bank_mask:0xf\n"
    "  s_mov_b64 vcc, s[64:65]\n"
    "  v_cndmask_b32_dpp v168, v176, v184, vcc quad_perm:[2,3,0,1] row_mask:0xf bank_mask:0xf\n"
    "  v_mov_b32 v255, v255\n"
    "  v_cndmask_b32_dpp v169, v177, v185, vcc quad_perm:[2,3,0,1] row_mask:0xf bank_mask:0xf\n"
    "  v_mov_b32 v255, v255\n"
    "  v_cndmask_b32_dpp v170, v178, v186, vcc quad_perm:[2,3,0,1] row_mask:0xf bank_mask:0xf\n"
    "  s_waitcnt lgkmcnt(0)\n"
    "  v_mfma_f32_32x32x16_bf16 v[96:111], v[236:239], v[152:155], v[96:111]\n"
    "  v_cndmask_b32_dpp v171, v179, v187, vcc quad_perm:[2,3,0,1] row_mask:0xf bank_mask:0xf\n"
    "  v_mov_b32 v255, v255\n"
    "  v_cndmask_b32_dpp v172, v180, v188, vcc quad_perm:[2,3,0,1] row_mask:0xf bank_mask:0xf\n"
    "  v_mov_b32 v255, v255\n"
    "  v_cndmask_b32_dpp v173, v181, v189, vcc quad_perm:[2,3,0,1] row_mask:0xf bank_mask:0xf\n"
    "  v_mov_b32 v255, v255\n"
    "  v_cndmask_b32_dpp v174, v182, v190, vcc quad_perm:[2,3,0,1] row_mask:0xf bank_mask:0xf\n"
    "  v_mov_b32 v255, v255\n"
    "  v_cndmask_b32_dpp v175, v183, v191, vcc quad_perm:[2,3,0,1] row_mask:0xf bank_mask:0xf\n"
    "  v_mfma_f32_32x32x16_bf16 v[96:111], v[240:243], v[156:159], v[96:111]\n"
    "  buffer_store_dwordx4 v[160:163], v9, s[16:19], 0 offen offset:0 sc1\n"
    "  buffer_store_dwordx4 v[164:167], v9, s[16:19], 0 offen offset:256 sc1\n"
    "  buffer_store_dwordx4 v[168:171], v9, s[16:19], 0 offen offset:512 sc1\n"
    "  buffer_store_dwordx4 v[172:175], v9, s[16:19], 0 offen offset:768 sc1\n"
    "  s_cmp_gt_u32 s26, 3\n"
    "  s_cbranch_scc1 .Lgqa_bepi\n"
    "  v_subrev_u32 v1, s31, v1\n"
    "  v_subrev_u32 v2, s31, v2\n"
    "  v_subrev_u32 v3, s31, v3\n"
    "  v_subrev_u32 v4, s31, v4\n"
    "  v_add_u32 v5, s40, v5\n"
    "  v_add_u32 v6, s40, v6\n"
    "  v_add_u32 v7, s41, v7\n"
    "  v_add_u32 v8, s41, v8\n"
    "  v_mov_b32 v244, 0xff7fffff\n"
    "  v_mov_b32 v246, 0\n"
    "  s_setprio 3\n"
    "  ds_read_b128 v[224:227], v1 offset:0\n"
    "  ds_read_b128 v[228:231], v2 offset:0\n"
    "  ds_read_b128 v[232:235], v3 offset:0\n"
    "  ds_read_b128 v[236:239], v4 offset:0\n"
    "  s_waitcnt vmcnt(4)\n"
    "  s_waitcnt lgkmcnt(2)\n"
    "  v_mfma_f32_32x32x16_bf16 v[128:143], v[224:227], v[112:115], 0\n"
    "  ds_read_b128 v[240:243], v1 offset:1024\n"
    "  v_mfma_f32_32x32x16_bf16 v[128:143], v[228:231], v[116:119], v[128:143]\n"
    "  ds_read_b128 v[224:227], v2 offset:1024\n"
    "  s_waitcnt lgkmcnt(2)\n"
    "  v_mfma_f32_32x32x16_bf16 v[128:143], v[232:235], v[120:123], v[128:143]\n"
    "  ds_read_b128 v[228:231], v3 offset:1024\n"
    "  v_mul_f32 v80, v80, v249\n"
    "  v_mul_f32 v81, v81, v249\n"
    "  v_cvt_pk_bf16_f32 v16, v80, v81\n"
    "  v_mul_f32 v88, v88, v249\n"
    "  v_mfma_f32_32x32x16_bf16 v[128:143], v[236:239], v[124:127], v[128:143]\n"
    "  ds_read_b128 v[232:235], v4 offset:1024\n"
    "  v_mul_f32 v89, v89, v249\n"
    "  v_cvt_pk_bf16_f32 v20, v88, v89\n"
    "  v_mul_f32 v96, v96, v249\n"
    "  s_waitcnt lgkmcnt(2)\n"
    "  v_mfma_f32_32x32x16_bf16 v[128:143], v[240:243], v[208:211], v[128:143]\n"
    "  ds_read_b128 v[236:239], v1 offset:8192\n"
    "  v_mul_f32 v97, v97, v249\n"
    "  v_cvt_pk_bf16_f32 v24, v96, v97\n"
    "  v_mul_f32 v104, v104, v249\n"
    "  v_mfma_f32_32x32x16_bf16 v[128:143], v[224:227], v[212:215], v[128:143]\n"
    "  ds_read_b128 v[240:243], v2 offset:8192\n"
    "  v_mul_f32 v105, v105, v249\n"
    "  v_cvt_pk_bf16_f32 v28, v104, v105\n"
    "  v_mul_f32 v82, v82, v249\n"
    "  s_waitcnt lgkmcnt(2)\n"
    "  v_mfma_f32_32x32x16_bf16 v[128:143], v[228:231], v[48:51], v[128:143]\n"
    "  ds_read_b128 v[224:227], v3 offset:8192\n"
    "  v_mul_f32 v83, v83, v249\n"
    "  v_cvt_pk_bf16_f32 v17, v82, v83\n"
    "  v_mul_f32 v90, v90, v249\n"
    "  v_mfma_f32_32x32x16_bf16 v[128:143], v[232:235], v[52:55], v[128:143]\n"
    "  ds_read_b128 v[228:231], v4 offset:8192\n"
    "  v_mul_f32 v91, v91, v249\n"
    "  v_cvt_pk_bf16_f32 v21, v90, v91\n"
    "  v_mul_f32 v98, v98, v249\n"
    "  s_waitcnt lgkmcnt(2)\n"
    "  v_mfma_f32_32x32x16_bf16 v[144:159], v[236:239], v[112:115], 0\n"
    "  ds_read_b128 v[232:235], v1 offset:9216\n"
    "  v_mul_f32 v99, v99, v249\n"
    "  v_cvt_pk_bf16_f32 v25, v98, v99\n"
    "  v_mul_f32 v106, v106, v249\n"
    "  v_mfma_f32_32x32x16_bf16 v[144:159], v[240:243], v[116:119], v[144:159]\n"
    "  ds_read_b128 v[236:239], v2 offset:9216\n"
    "  v_mul_f32 v107, v107, v249\n"
    "  v_cvt_pk_bf16_f32 v29, v106, v107\n"
    "  s_mov_b64 vcc, s[58:59]\n"
    "  s_waitcnt lgkmcnt(2)\n"
    "  v_mfma_f32_32x32x16_bf16 v[144:159], v[224:227], v[120:123], v[144:159]\n"
    "  ds_read_b128 v[240:243], v3 offset:9216\n"
    "  v_mul_f32 v84, v84, v249\n"
    "  v_mul_f32 v85, v85, v249\n"
    "  v_cvt_pk_bf16_f32 v18, v84, v85\n"
    "  v_mfma_f32_32x32x16_bf16 v[144:159], v[228:231], v[124:127], v[144:159]\n"
    "  ds_read_b128 v[224:227], v4 offset:9216\n"
    "  v_cndmask_b32_dpp v32, v20, v16, vcc quad_perm:[1,0,3,2] row_mask:0xf bank_mask:0xf\n"
    "  v_mul_f32 v92, v92, v249\n"
    "  v_mul_f32 v93, v93, v249\n"
    "  v_max3_f32 v244, v244, v128, v129\n"
    "  v_max3_f32 v244, v244, v130, v131\n"
    "  s_waitcnt lgkmcnt(2)\n"
    "  v_mfma_f32_32x32x16_bf16 v[144:159], v[232:235], v[208:211], v[144:159]\n"
    "  ds_read_b128 v[228:231], v1 offset:16384\n"
    "  v_cvt_pk_bf16_f32 v22, v92, v93\n"
    "  v_cndmask_b32_dpp v33, v21, v17, vcc quad_perm:[1,0,3,2] row_mask:0xf bank_mask:0xf\n"
    "  v_mul_f32 v100, v100, v249\n"
    "  v_max3_f32 v244, v244, v132, v133\n"
    "  v_max3_f32 v244, v244, v134, v135\n"
    "  v_mfma_f32_32x32x16_bf16 v[144:159], v[236:239], v[212:215], v[144:159]\n"
    "  ds_read_b128 v[232:235], v2 offset:16384\n"
    "  v_mul_f32 v101, v101, v249\n"
    "  v_cvt_pk_bf16_f32 v26, v100, v101\n"
    "  v_cndmask_b32_dpp v40, v28, v24, vcc quad_perm:[1,0,3,2] row_mask:0xf bank_mask:0xf\n"
    "  v_max3_f32 v244, v244, v136, v137\n"
    "  s_waitcnt lgkmcnt(2)\n"
    "  v_mfma_f32_32x32x16_bf16 v[144:159], v[240:243], v[48:51], v[144:159]\n"
    "  ds_read_b128 v[236:239], v3 offset:16384\n"
    "  v_mul_f32 v108, v108, v249\n"
    "  v_mul_f32 v109, v109, v249\n"
    "  v_cvt_pk_bf16_f32 v30, v108, v109\n"
    "  v_max3_f32 v244, v244, v138, v139\n"
    "  v_max3_f32 v244, v244, v140, v141\n"
    "  v_mfma_f32_32x32x16_bf16 v[144:159], v[224:227], v[52:55], v[144:159]\n"
    "  ds_read_b128 v[240:243], v4 offset:16384\n"
    "  v_cndmask_b32_dpp v41, v29, v25, vcc quad_perm:[1,0,3,2] row_mask:0xf bank_mask:0xf\n"
    "  v_mul_f32 v86, v86, v249\n"
    "  v_mul_f32 v87, v87, v249\n"
    "  v_max3_f32 v244, v244, v142, v143\n"
    "  s_waitcnt lgkmcnt(2)\n"
    "  v_mfma_f32_32x32x16_bf16 v[160:175], v[228:231], v[112:115], 0\n"
    "  ds_read_b128 v[224:227], v1 offset:17408\n"
    "  v_cvt_pk_bf16_f32 v19, v86, v87\n"
    "  s_mov_b64 vcc, s[60:61]\n"
    "  v_mul_f32 v94, v94, v249\n"
    "  v_mul_f32 v95, v95, v249\n"
    "  v_mfma_f32_32x32x16_bf16 v[160:175], v[232:235], v[116:119], v[160:175]\n"
    "  ds_read_b128 v[228:231], v2 offset:17408\n"
    "  v_cvt_pk_bf16_f32 v23, v94, v95\n"
    "  v_cndmask_b32_dpp v36, v16, v20, vcc quad_perm:[1,0,3,2] row_mask:0xf bank_mask:0xf\n"
    "  v_mul_f32 v102, v102, v249\n"
    "  s_waitcnt lgkmcnt(2)\n"
    "  v_mfma_f32_32x32x16_bf16 v[160:175], v[236:239], v[120:123], v[160:175]\n"
    "  ds_read_b128 v[232:235], v3 offset:17408\n"
    "  v_mul_f32 v103, v103, v249\n"
    "  v_cvt_pk_bf16_f32 v27, v102, v103\n"
    "  v_cndmask_b32_dpp v37, v17, v21, vcc quad_perm:[1,0,3,2] row_mask:0xf bank_mask:0xf\n"
    "  v_mfma_f32_32x32x16_bf16 v[160:175], v[240:243], v[124:127], v[160:175]\n"
    "  ds_read_b128 v[236:239], v4 offset:17408\n"
    "  v_mul_f32 v110, v110, v249\n"
    "  v_mul_f32 v111, v111, v249\n"
    "  v_cvt_pk_bf16_f32 v31, v110, v111\n"
    "  v_max3_f32 v244, v244, v144, v145\n"
    "  v_max3_f32 v244, v244, v146, v147\n"
    "  s_waitcnt lgkmcnt(2)\n"
    "  v_mfma_f32_32x32x16_bf16 v[160:175], v[224:227], v[208:211], v[160:175]\n"
    "  ds_read_b128 v[240:243], v1 offset:24576\n"
    "  v_cndmask_b32_dpp v44, v24, v28, vcc quad_perm:[1,0,3,2] row_mask:0xf bank_mask:0xf\n"
    "  v_mov_b32 v255, v255\n"
    "  v_cndmask_b32_dpp v45, v25, v29, vcc quad_perm:[1,0,3,2] row_mask:0xf bank_mask:0xf\n"
    "  v_mov_b32 v255, v255\n"
    "  v_cndmask_b32_dpp v38, v18, v22, vcc quad_perm:[1,0,3,2] row_mask:0xf bank_mask:0xf\n"
    "  v_max3_f32 v244, v244, v148, v149\n"
    "  v_max3_f32 v244, v244, v150, v151\n"
    "  v_mfma_f32_32x32x16_bf16 v[160:175], v[228:231], v[212:215], v[160:175]\n"
    "  ds_read_b128 v[224:227], v2 offset:24576\n"
    "  v_cndmask_b32_dpp v39, v19, v23, vcc quad_perm:[1,0,3,2] row_mask:0xf bank_mask:0xf\n"
    "  v_mov_b32 v255, v255\n"
    "  v_cndmask_b32_dpp v46, v26, v30, vcc quad_perm:[1,0,3,2] row_mask:0xf bank_mask:0xf\n"
    "  v_mov_b32 v255, v255\n"
    "  v_cndmask_b32_dpp v47, v27, v31, vcc quad_perm:[1,0,3,2] row_mask:0xf bank_mask:0xf\n"
    "  v_max3_f32 v244, v244, v152, v153\n"
    "  s_waitcnt lgkmcnt(2)\n"
    "  v_mfma_f32_32x32x16_bf16 v[160:175], v[232:235], v[48:51], v[160:175]\n"
    "  ds_read_b128 v[228:231], v3 offset:24576\n"
    "  s_mov_b64 vcc, s[58:59]\n"
    "  v_cndmask_b32_dpp v34, v22, v18, vcc quad_perm:[1,0,3,2] row_mask:0xf bank_mask:0xf\n"
    "  v_mov_b32 v255, v255\n"
    "  v_cndmask_b32_dpp v35, v23, v19, vcc quad_perm:[1,0,3,2] row_mask:0xf bank_mask:0xf\n"
    "  v_max3_f32 v244, v244, v154, v155\n"
    "  v_max3_f32 v244, v244, v156, v157\n"
    "  v_mfma_f32_32x32x16_bf16 v[160:175], v[236:239], v[52:55], v[160:175]\n"
    "  ds_read_b128 v[232:235], v4 offset:24576\n"
    "  v_cndmask_b32_dpp v42, v30, v26, vcc quad_perm:[1,0,3,2] row_mask:0xf bank_mask:0xf\n"
    "  v_mov_b32 v255, v255\n"
    "  v_cndmask_b32_dpp v43, v31, v27, vcc quad_perm:[1,0,3,2] row_mask:0xf bank_mask:0xf\n"
    "  s_mov_b64 vcc, s[62:63]\n"
    "  v_max3_f32 v244, v244, v158, v159\n"
    "  s_waitcnt lgkmcnt(2)\n"
    "  v_mfma_f32_32x32x16_bf16 v[176:191], v[240:243], v[112:115], 0\n"
    "  ds_read_b128 v[236:239], v1 offset:25600\n"
    "  v_cndmask_b32_dpp v16, v40, v32, vcc quad_perm:[2,3,0,1] row_mask:0xf bank_mask:0xf\n"
    "  v_mov_b32 v255, v255\n"
    "  v_cndmask_b32_dpp v17, v41, v33, vcc quad_perm:[2,3,0,1] row_mask:0xf bank_mask:0xf\n"
    "  v_mov_b32 v255, v255\n"
    "  v_cndmask_b32_dpp v18, v42, v34, vcc quad_perm:[2,3,0,1] row_mask:0xf bank_mask:0xf\n"
    "  v_mfma_f32_32x32x16_bf16 v[176:191], v[224:227], v[116:119], v[176:191]\n"
    "  ds_read_b128 v[240:243], v2 offset:25600\n"
    "  v_cndmask_b32_dpp v19, v43, v35, vcc quad_perm:[2,3,0,1] row_mask:0xf bank_mask:0xf\n"
    "  v_mov_b32 v255, v255\n"
    "  v_cndmask_b32_dpp v20, v44, v36, vcc quad_perm:[2,3,0,1] row_mask:0xf bank_mask:0xf\n"
    "  v_mov_b32 v255, v255\n"
    "  v_cndmask_b32_dpp v21, v45, v37, vcc quad_perm:[2,3,0,1] row_mask:0xf bank_mask:0xf\n"
    "  s_waitcnt lgkmcnt(2)\n"
    "  v_mfma_f32_32x32x16_bf16 v[176:191], v[228:231], v[120:123], v[176:191]\n"
    "  ds_read_b128 v[224:227], v3 offset:25600\n"
    "  v_cndmask_b32_dpp v22, v46, v38, vcc quad_perm:[2,3,0,1] row_mask:0xf bank_mask:0xf\n"
    "  v_mov_b32 v255, v255\n"
    "  v_cndmask_b32_dpp v23, v47, v39, vcc quad_perm:[2,3,0,1] row_mask:0xf bank_mask:0xf\n"
    "  s_mov_b64 vcc, s[64:65]\n"
    "  v_mfma_f32_32x32x16_bf16 v[176:191], v[232:235], v[124:127], v[176:191]\n"
    "  ds_read_b128 v[228:231], v4 offset:25600\n"
    "  v_cndmask_b32_dpp v24, v32, v40, vcc quad_perm:[2,3,0,1] row_mask:0xf bank_mask:0xf\n"
    "  v_mov_b32 v255, v255\n"
    "  v_cndmask_b32_dpp v25, v33, v41, vcc quad_perm:[2,3,0,1] row_mask:0xf bank_mask:0xf\n"
    "  v_mov_b32 v255, v255\n"
    "  v_cndmask_b32_dpp v26, v34, v42, vcc quad_perm:[2,3,0,1] row_mask:0xf bank_mask:0xf\n"
    "  v_max3_f32 v244, v244, v160, v161\n"
    "  v_max3_f32 v244, v244, v162, v163\n"
    "  s_waitcnt lgkmcnt(2)\n"
    "  v_mfma_f32_32x32x16_bf16 v[176:191], v[236:239], v[208:211], v[176:191]\n"
    "  ds_read_b128 v[232:235], v1 offset:32768\n"
    "  v_cndmask_b32_dpp v27, v35, v43, vcc quad_perm:[2,3,0,1] row_mask:0xf bank_mask:0xf\n"
    "  v_mov_b32 v255, v255\n"
    "  v_cndmask_b32_dpp v28, v36, v44, vcc quad_perm:[2,3,0,1] row_mask:0xf bank_mask:0xf\n"
    "  v_mov_b32 v255, v255\n"
    "  v_cndmask_b32_dpp v29, v37, v45, vcc quad_perm:[2,3,0,1] row_mask:0xf bank_mask:0xf\n"
    "  v_max3_f32 v244, v244, v164, v165\n"
    "  v_max3_f32 v244, v244, v166, v167\n"
    "  v_mfma_f32_32x32x16_bf16 v[176:191], v[240:243], v[212:215], v[176:191]\n"
    "  ds_read_b128 v[236:239], v2 offset:32768\n"
    "  v_cndmask_b32_dpp v30, v38, v46, vcc quad_perm:[2,3,0,1] row_mask:0xf bank_mask:0xf\n"
    "  v_mov_b32 v255, v255\n"
    "  v_cndmask_b32_dpp v31, v39, v47, vcc quad_perm:[2,3,0,1] row_mask:0xf bank_mask:0xf\n"
    "  buffer_store_dwordx4 v[16:19], v9, s[16:19], 0 offen offset:128 sc1\n"
    "  v_max3_f32 v244, v244, v168, v169\n"
    "  s_waitcnt lgkmcnt(2)\n"
    "  v_mfma_f32_32x32x16_bf16 v[176:191], v[224:227], v[48:51], v[176:191]\n"
    "  ds_read_b128 v[240:243], v3 offset:32768\n"
    "  buffer_store_dwordx4 v[20:23], v9, s[16:19], 0 offen offset:384 sc1\n"
    "  buffer_store_dwordx4 v[24:27], v9, s[16:19], 0 offen offset:640 sc1\n"
    "  buffer_store_dwordx4 v[28:31], v9, s[16:19], 0 offen offset:896 sc1\n"
    "  v_max3_f32 v244, v244, v170, v171\n"
    "  v_max3_f32 v244, v244, v172, v173\n"
    "  v_mfma_f32_32x32x16_bf16 v[176:191], v[228:231], v[52:55], v[176:191]\n"
    "  ds_read_b128 v[224:227], v4 offset:32768\n"
    "  v_max3_f32 v244, v244, v174, v175\n"
    "  s_waitcnt lgkmcnt(2)\n"
    "  v_mfma_f32_32x32x16_bf16 v[192:207], v[232:235], v[112:115], 0\n"
    "  ds_read_b128 v[228:231], v1 offset:33792\n"
    "  v_mfma_f32_32x32x16_bf16 v[192:207], v[236:239], v[116:119], v[192:207]\n"
    "  ds_read_b128 v[232:235], v2 offset:33792\n"
    "  s_waitcnt lgkmcnt(2)\n"
    "  v_mfma_f32_32x32x16_bf16 v[192:207], v[240:243], v[120:123], v[192:207]\n"
    "  ds_read_b128 v[236:239], v3 offset:33792\n"
    "  v_mfma_f32_32x32x16_bf16 v[192:207], v[224:227], v[124:127], v[192:207]\n"
    "  ds_read_b128 v[240:243], v4 offset:33792\n"
    "  v_max3_f32 v244, v244, v176, v177\n"
    "  v_max3_f32 v244, v244, v178, v179\n"
    "  s_waitcnt lgkmcnt(2)\n"
    "  v_mfma_f32_32x32x16_bf16 v[192:207], v[228:231], v[208:211], v[192:207]\n"
    "  ds_read_b64_tr_b16 v[224:225], v7 offset:0\n"
    "  ds_read_b64_tr_b16 v[226:227], v7 offset:2048\n"
    "  v_max3_f32 v244, v244, v180, v181\n"
    "  v_max3_f32 v244, v244, v182, v183\n"
    "  v_mfma_f32_32x32x16_bf16 v[192:207], v[232:235], v[212:215], v[192:207]\n"
    "  ds_read_b64_tr_b16 v[228:229], v7 offset:4096\n"
    "  ds_read_b64_tr_b16 v[230:231], v7 offset:6144\n"
    "  v_max3_f32 v244, v244, v184, v185\n"
    "  s_waitcnt lgkmcnt(4)\n"
    "  v_mfma_f32_32x32x16_bf16 v[192:207], v[236:239], v[48:51], v[192:207]\n"
    "  ds_read_b64_tr_b16 v[232:233], v7 offset:8192\n"
    "  ds_read_b64_tr_b16 v[234:235], v7 offset:10240\n"
    "  v_max3_f32 v244, v244, v186, v187\n"
    "  v_max3_f32 v244, v244, v188, v189\n"
    "  v_mfma_f32_32x32x16_bf16 v[192:207], v[240:243], v[52:55], v[192:207]\n"
    "  ds_read_b64_tr_b16 v[236:237], v7 offset:12288\n"
    "  ds_read_b64_tr_b16 v[238:239], v7 offset:14336\n"
    "  v_max3_f32 v244, v244, v190, v191\n"
    "  s_setprio 1\n"
    "  s_lshl_b32 s77, s26, 2\n"
    "  v_mov_b32 v112, 1\n"
    "  v_lshlrev_b32 v113, 10, v13\n"
    "  buffer_store_dword v112, v113, s[20:23], s77 offen offset:32\n"
    "  s_nop 3\n"
    "  v_max3_f32 v244, v244, v192, v193\n"
    "  v_max3_f32 v244, v244, v194, v195\n"
    "  v_max3_f32 v244, v244, v196, v197\n"
    "  v_max3_f32 v244, v244, v198, v199\n"
    "  v_max3_f32 v244, v244, v200, v201\n"
    "  v_max3_f32 v244, v244, v202, v203\n"
    "  v_max3_f32 v244, v244, v204, v205\n"
    "  v_max3_f32 v244, v244, v206, v207\n"
    "  v_mov_b32 v250, v244\n"
    "  s_nop 1\n"
    "  v_permlane32_swap_b32 v250, v244\n"
    "  v_max_f32 v244, v244, v250\n"
    "  v_mul_f32 v247, 0x3e0293ee, v244\n"
    "  v_sub_f32 v247, 0, v247\n"
    "  v_mov_b32 v245, 0\n"
    "  v_fma_f32 v250, v128, v248, v247\n"
    "  v_fma_f32 v251, v129, v248, v247\n"
    "  v_fma_f32 v252, v130, v248, v247\n"
    "  v_fma_f32 v253, v131, v248, v247\n"
    "  v_exp_f32 v250, v250\n"
    "  v_exp_f32 v251, v251\n"
    "  v_fma_f32 v254, v132, v248, v247\n"
    "  v_fma_f32 v255, v133, v248, v247\n"
    "  v_add_f32 v246, v246, v250\n"
    "  v_cvt_pk_bf16_f32 v216, v250, v251\n"
    "  v_add_f32 v246, v246, v251\n"
    "  v_exp_f32 v252, v252\n"
    "  v_exp_f32 v253, v253\n"
    "  v_fma_f32 v250, v134, v248, v247\n"
    "  v_fma_f32 v251, v135, v248, v247\n"
    "  v_add_f32 v245, v245, v252\n"
    "  v_cvt_pk_bf16_f32 v217, v252, v253\n"
    "  v_add_f32 v245, v245, v253\n"
    "  v_exp_f32 v254, v254\n"
    "  v_exp_f32 v255, v255\n"
    "  v_fma_f32 v252, v136, v248, v247\n"
    "  v_fma_f32 v253, v137, v248, v247\n"
    "  v_add_f32 v246, v246, v254\n"
    "  v_cvt_pk_bf16_f32 v218, v254, v255\n"
    "  v_add_f32 v246, v246, v255\n"
    "  v_exp_f32 v250, v250\n"
    "  v_exp_f32 v251, v251\n"
    "  v_fma_f32 v254, v138, v248, v247\n"
    "  v_fma_f32 v255, v139, v248, v247\n"
    "  v_add_f32 v245, v245, v250\n"
    "  v_cvt_pk_bf16_f32 v219, v250, v251\n"
    "  v_add_f32 v245, v245, v251\n"
    "  v_exp_f32 v252, v252\n"
    "  v_exp_f32 v253, v253\n"
    "  v_fma_f32 v250, v140, v248, v247\n"
    "  v_fma_f32 v251, v141, v248, v247\n"
    "  v_add_f32 v246, v246, v252\n"
    "  v_cvt_pk_bf16_f32 v220, v252, v253\n"
    "  v_add_f32 v246, v246, v253\n"
    "  v_exp_f32 v254, v254\n"
    "  v_exp_f32 v255, v255\n"
    "  v_fma_f32 v252, v142, v248, v247\n"
    "  v_fma_f32 v253, v143, v248, v247\n"
    "  v_add_f32 v245, v245, v254\n"
    "  v_cvt_pk_bf16_f32 v221, v254, v255\n"
    "  v_add_f32 v245, v245, v255\n"
    "  v_exp_f32 v250, v250\n"
    "  v_exp_f32 v251, v251\n"
    "  v_fma_f32 v254, v144, v248, v247\n"
    "  v_fma_f32 v255, v145, v248, v247\n"
    "  v_add_f32 v246, v246, v250\n"
    "  v_cvt_pk_bf16_f32 v222, v250, v251\n"
    "  v_add_f32 v246, v246, v251\n"
    "  v_exp_f32 v252, v252\n"
    "  v_exp_f32 v253, v253\n"
    "  v_fma_f32 v250, v146, v248, v247\n"
    "  v_fma_f32 v251, v147, v248, v247\n"
    "  v_add_f32 v245, v245, v252\n"
    "  v_cvt_pk_bf16_f32 v223, v252, v253\n"
    "  v_add_f32 v245, v245, v253\n"
    "  v_exp_f32 v254, v254\n"
    "  v_exp_f32 v255, v255\n"
    "  v_fma_f32 v252, v148, v248, v247\n"
    "  v_fma_f32 v253, v149, v248, v247\n"
    "  v_add_f32 v246, v246, v254\n"
    "  v_cvt_pk_bf16_f32 v128, v254, v255\n"
    "  v_add_f32 v246, v246, v255\n"
    "  v_exp_f32 v250, v250\n"
    "  v_exp_f32 v251, v251\n"
    "  v_fma_f32 v254, v150, v248, v247\n"
    "  v_fma_f32 v255, v151, v248, v247\n"
    "  v_add_f32 v245, v245, v250\n"
    "  v_cvt_pk_bf16_f32 v129, v250, v251\n"
    "  v_add_f32 v245, v245, v251\n"
    "  v_exp_f32 v252, v252\n"
    "  v_exp_f32 v253, v253\n"
    "  v_fma_f32 v250, v152, v248, v247\n"
    "  v_fma_f32 v251, v153, v248, v247\n"
    "  v_add_f32 v246, v246, v252\n"
    "  v_cvt_pk_bf16_f32 v130, v252, v253\n"
    "  v_add_f32 v246, v246, v253\n"
    "  v_exp_f32 v254, v254\n"
    "  v_exp_f32 v255, v255\n"
    "  v_fma_f32 v252, v154, v248, v247\n"
    "  v_fma_f32 v253, v155, v248, v247\n"
    "  v_add_f32 v245, v245, v254\n"
    "  v_cvt_pk_bf16_f32 v131, v254, v255\n"
    "  v_add_f32 v245, v245, v255\n"
    "  v_exp_f32 v250, v250\n"
    "  v_exp_f32 v251, v251\n"
    "  v_fma_f32 v254, v156, v248, v247\n"
    "  v_fma_f32 v255, v157, v248, v247\n"
    "  v_add_f32 v246, v246, v250\n"
    "  v_cvt_pk_bf16_f32 v132, v250, v251\n"
    "  v_add_f32 v246, v246, v251\n"
    "  v_exp_f32 v252, v252\n"
    "  v_exp_f32 v253, v253\n"
    "  v_fma_f32 v250, v158, v248, v247\n"
    "  v_fma_f32 v251, v159, v248, v247\n"
    "  v_add_f32 v245, v245, v252\n"
    "  v_cvt_pk_bf16_f32 v133, v252, v253\n"
    "  v_add_f32 v245, v245, v253\n"
    "  v_exp_f32 v254, v254\n"
    "  v_exp_f32 v255, v255\n"
    "  v_fma_f32 v252, v160, v248, v247\n"
    "  v_fma_f32 v253, v161, v248, v247\n"
    "  v_add_f32 v246, v246, v254\n"
    "  v_cvt_pk_bf16_f32 v134, v254, v255\n"
    "  v_add_f32 v246, v246, v255\n"
    "  v_exp_f32 v250, v250\n"
    "  v_exp_f32 v251, v251\n"
    "  v_fma_f32 v254, v162, v248, v247\n"
    "  v_fma_f32 v255, v163, v248, v247\n"
    "  v_add_f32 v245, v245, v250\n"
    "  v_cvt_pk_bf16_f32 v135, v250, v251\n"
    "  v_add_f32 v245, v245, v251\n"
    "  v_exp_f32 v252, v252\n"
    "  v_exp_f32 v253, v253\n"
    "  v_fma_f32 v250, v164, v248, v247\n"
    "  v_fma_f32 v251, v165, v248, v247\n"
    "  v_add_f32 v246, v246, v252\n"
    "  v_cvt_pk_bf16_f32 v136, v252, v253\n"
    "  v_add_f32 v246, v246, v253\n"
    "  v_exp_f32 v254, v254\n"
    "  v_exp_f32 v255, v255\n"
    "  v_fma_f32 v252, v166, v248, v247\n"
    "  v_fma_f32 v253, v167, v248, v247\n"
    "  v_add_f32 v245, v245, v254\n"
    "  v_cvt_pk_bf16_f32 v137, v254, v255\n"
    "  v_add_f32 v245, v245, v255\n"
    "  v_exp_f32 v250, v250\n"
    "  v_exp_f32 v251, v251\n"
    "  v_fma_f32 v254, v168, v248, v247\n"
    "  v_fma_f32 v255, v169, v248, v247\n"
    "  v_add_f32 v246, v246, v250\n"
    "  v_cvt_pk_bf16_f32 v138, v250, v251\n"
    "  v_add_f32 v246, v246, v251\n"
    "  v_exp_f32 v252, v252\n"
    "  v_exp_f32 v253, v253\n"
    "  v_fma_f32 v250, v170, v248, v247\n"
    "  v_fma_f32 v251, v171, v248, v247\n"
    "  v_add_f32 v245, v245, v252\n"
    "  v_cvt_pk_bf16_f32 v139, v252, v253\n"
    "  v_add_f32 v245, v245, v253\n"
    "  v_exp_f32 v254, v254\n"
    "  v_exp_f32 v255, v255\n"
    "  v_fma_f32 v252, v172, v248, v247\n"
    "  v_fma_f32 v253, v173, v248, v247\n"
    "  v_add_f32 v246, v246, v254\n"
    "  v_cvt_pk_bf16_f32 v140, v254, v255\n"
    "  v_add_f32 v246, v246, v255\n"
    "  v_exp_f32 v250, v250\n"
    "  v_exp_f32 v251, v251\n"
    "  v_fma_f32 v254, v174, v248, v247\n"
    "  v_fma_f32 v255, v175, v248, v247\n"
    "  v_add_f32 v245, v245, v250\n"
    "  v_cvt_pk_bf16_f32 v141, v250, v251\n"
    "  v_add_f32 v245, v245, v251\n"
    "  v_exp_f32 v252, v252\n"
    "  v_exp_f32 v253, v253\n"
    "  v_fma_f32 v250, v176, v248, v247\n"
    "  v_fma_f32 v251, v177, v248, v247\n"
    "  v_add_f32 v246, v246, v252\n"
    "  v_cvt_pk_bf16_f32 v142, v252, v253\n"
    "  v_add_f32 v246, v246, v253\n"
    "  v_exp_f32 v254, v254\n"
    "  v_exp_f32 v255, v255\n"
    "  v_fma_f32 v252, v178, v248, v247\n"
    "  v_fma_f32 v253, v179, v248, v247\n"
    "  v_add_f32 v245, v245, v254\n"
    "  v_cvt_pk_bf16_f32 v143, v254, v255\n"
    "  v_add_f32 v245, v245, v255\n"
    "  v_exp_f32 v250, v250\n"
    "  v_exp_f32 v251, v251\n"
    "  v_fma_f32 v254, v180, v248, v247\n"
    "  v_fma_f32 v255, v181, v248, v247\n"
    "  v_add_f32 v246, v246, v250\n"
    "  v_cvt_pk_bf16_f32 v144, v250, v251\n"
    "  v_add_f32 v246, v246, v251\n"
    "  v_exp_f32 v252, v252\n"
    "  v_exp_f32 v253, v253\n"
    "  v_fma_f32 v250, v182, v248, v247\n"
    "  v_fma_f32 v251, v183, v248, v247\n"
    "  v_add_f32 v245, v245, v252\n"
    "  v_cvt_pk_bf16_f32 v145, v252, v253\n"
    "  v_add_f32 v245, v245, v253\n"
    "  v_exp_f32 v254, v254\n"
    "  v_exp_f32 v255, v255\n"
    "  v_fma_f32 v252, v184, v248, v247\n"
    "  v_fma_f32 v253, v185, v248, v247\n"
    "  v_add_f32 v246, v246, v254\n"
    "  v_cvt_pk_bf16_f32 v146, v254, v255\n"
    "  v_add_f32 v246, v246, v255\n"
    "  v_exp_f32 v250, v250\n"
    "  v_exp_f32 v251, v251\n"
    "  v_fma_f32 v254, v186, v248, v247\n"
    "  v_fma_f32 v255, v187, v248, v247\n"
    "  v_add_f32 v245, v245, v250\n"
    "  v_cvt_pk_bf16_f32 v147, v250, v251\n"
    "  v_add_f32 v245, v245, v251\n"
    "  v_exp_f32 v252, v252\n"
    "  v_exp_f32 v253, v253\n"
    "  v_fma_f32 v250, v188, v248, v247\n"
    "  v_fma_f32 v251, v189, v248, v247\n"
    "  v_add_f32 v246, v246, v252\n"
    "  v_cvt_pk_bf16_f32 v148, v252, v253\n"
    "  v_add_f32 v246, v246, v253\n"
    "  v_exp_f32 v254, v254\n"
    "  v_exp_f32 v255, v255\n"
    "  v_fma_f32 v252, v190, v248, v247\n"
    "  v_fma_f32 v253, v191, v248, v247\n"
    "  v_add_f32 v245, v245, v254\n"
    "  v_cvt_pk_bf16_f32 v149, v254, v255\n"
    "  v_add_f32 v245, v245, v255\n"
    "  v_exp_f32 v250, v250\n"
    "  v_exp_f32 v251, v251\n"
    "  v_fma_f32 v254, v192, v248, v247\n"
    "  v_fma_f32 v255, v193, v248, v247\n"
    "  v_add_f32 v246, v246, v250\n"
    "  v_cvt_pk_bf16_f32 v150, v250, v251\n"
    "  v_add_f32 v246, v246, v251\n"
    "  v_exp_f32 v252, v252\n"
    "  v_exp_f32 v253, v253\n"
    "  v_fma_f32 v250, v194, v248, v247\n"
    "  v_fma_f32 v251, v195, v248, v247\n"
    "  v_add_f32 v245, v245, v252\n"
    "  v_cvt_pk_bf16_f32 v151, v252, v253\n"
    "  v_add_f32 v245, v245, v253\n"
    "  v_exp_f32 v254, v254\n"
    "  v_exp_f32 v255, v255\n"
    "  v_fma_f32 v252, v196, v248, v247\n"
    "  v_fma_f32 v253, v197, v248, v247\n"
    "  v_add_f32 v246, v246, v254\n"
    "  v_cvt_pk_bf16_f32 v152, v254, v255\n"
    "  v_add_f32 v246, v246, v255\n"
    "  v_exp_f32 v250, v250\n"
    "  v_exp_f32 v251, v251\n"
    "  v_fma_f32 v254, v198, v248, v247\n"
    "  v_fma_f32 v255, v199, v248, v247\n"
    "  v_add_f32 v245, v245, v250\n"
    "  v_cvt_pk_bf16_f32 v153, v250, v251\n"
    "  v_add_f32 v245, v245, v251\n"
    "  v_exp_f32 v252, v252\n"
    "  v_exp_f32 v253, v253\n"
    "  v_fma_f32 v250, v200, v248, v247\n"
    "  v_fma_f32 v251, v201, v248, v247\n"
    "  v_add_f32 v246, v246, v252\n"
    "  v_cvt_pk_bf16_f32 v154, v252, v253\n"
    "  v_add_f32 v246, v246, v253\n"
    "  v_exp_f32 v254, v254\n"
    "  v_exp_f32 v255, v255\n"
    "  v_fma_f32 v252, v202, v248, v247\n"
    "  v_fma_f32 v253, v203, v248, v247\n"
    "  v_add_f32 v245, v245, v254\n"
    "  v_cvt_pk_bf16_f32 v155, v254, v255\n"
    "  v_add_f32 v245, v245, v255\n"
    "  v_exp_f32 v250, v250\n"
    "  v_exp_f32 v251, v251\n"
    "  v_fma_f32 v254, v204, v248, v247\n"
    "  v_fma_f32 v255, v205, v248, v247\n"
    "  v_add_f32 v246, v246, v250\n"
    "  v_cvt_pk_bf16_f32 v156, v250, v251\n"
    "  v_add_f32 v246, v246, v251\n"
    "  v_exp_f32 v252, v252\n"
    "  v_exp_f32 v253, v253\n"
    "  v_fma_f32 v250, v206, v248, v247\n"
    "  v_fma_f32 v251, v207, v248, v247\n"
    "  v_add_f32 v245, v245, v252\n"
    "  v_cvt_pk_bf16_f32 v157, v252, v253\n"
    "  v_add_f32 v245, v245, v253\n"
    "  v_exp_f32 v254, v254\n"
    "  v_exp_f32 v255, v255\n"
    "  v_add_f32 v246, v246, v254\n"
    "  v_cvt_pk_bf16_f32 v158, v254, v255\n"
    "  v_add_f32 v246, v246, v255\n"
    "  v_exp_f32 v250, v250\n"
    "  v_exp_f32 v251, v251\n"
    "  v_add_f32 v245, v245, v250\n"
    "  v_cvt_pk_bf16_f32 v159, v250, v251\n"
    "  v_add_f32 v245, v245, v251\n"
    "  v_add_f32 v246, v245, v246\n"
    "  v_mov_b32 v250, v246\n"
    "  s_nop 1\n"
    "  v_permlane32_swap_b32 v250, v246\n"
    "  v_add_f32 v246, v246, v250\n"
    "  v_lshl_add_u32 v114, v13, 2, s43\n"
    "  v_add_u32 v9, s44, v114\n"
    "  s_setprio 3\n"
    "  s_waitcnt lgkmcnt(4)\n"
    "  v_mfma_f32_32x32x16_bf16 v[80:95], v[224:227], v[216:219], 0\n"
    "  ds_read_b64_tr_b16 v[240:241], v7 offset:16384\n"
    "  ds_read_b64_tr_b16 v[242:243], v7 offset:18432\n"
    "  v_mfma_f32_32x32x16_bf16 v[80:95], v[228:231], v[220:223], v[80:95]\n"
    "  ds_read_b64_tr_b16 v[224:225], v7 offset:20480\n"
    "  ds_read_b64_tr_b16 v[226:227], v7 offset:22528\n"
    "  s_waitcnt lgkmcnt(4)\n"
    "  v_mfma_f32_32x32x16_bf16 v[80:95], v[232:235], v[128:131], v[80:95]\n"
    "  ds_read_b64_tr_b16 v[228:229], v7 offset:24576\n"
    "  ds_read_b64_tr_b16 v[230:231], v7 offset:26624\n"
    "  v_mfma_f32_32x32x16_bf16 v[80:95], v[236:239], v[132:135], v[80:95]\n"
    "  ds_read_b64_tr_b16 v[232:233], v7 offset:28672\n"
    "  ds_read_b64_tr_b16 v[234:235], v7 offset:30720\n"
    "  s_waitcnt lgkmcnt(4)\n"
    "  v_mfma_f32_32x32x16_bf16 v[80:95], v[240:243], v[136:139], v[80:95]\n"
    "  ds_read_b64_tr_b16 v[236:237], v7 offset:32768\n"
    "  ds_read_b64_tr_b16 v[238:239], v7 offset:34816\n"
    "  v_mfma_f32_32x32x16_bf16 v[80:95], v[224:227], v[140:143], v[80:95]\n"
    "  ds_read_b64_tr_b16 v[240:241], v7 offset:36864\n"
    "  ds_read_b64_tr_b16 v[242:243], v7 offset:38912\n"
    "  s_waitcnt lgkmcnt(4)\n"
    "  v_mfma_f32_32x32x16_bf16 v[80:95], v[228:231], v[144:147], v[80:95]\n"
    "  ds_read_b64_tr_b16 v[224:225], v8 offset:0\n"
    "  ds_read_b64_tr_b16 v[226:227], v8 offset:2048\n"
    "  v_mfma_f32_32x32x16_bf16 v[80:95], v[232:235], v[148:151], v[80:95]\n"
    "  ds_read_b64_tr_b16 v[228:229], v8 offset:4096\n"
    "  ds_read_b64_tr_b16 v[230:231], v8 offset:6144\n"
    "  s_waitcnt lgkmcnt(4)\n"
    "  v_mfma_f32_32x32x16_bf16 v[80:95], v[236:239], v[152:155], v[80:95]\n"
    "  ds_read_b64_tr_b16 v[232:233], v8 offset:8192\n"
    "  ds_read_b64_tr_b16 v[234:235], v8 offset:10240\n"
    "  v_mfma_f32_32x32x16_bf16 v[80:95], v[240:243], v[156:159], v[80:95]\n"
    "  ds_read_b64_tr_b16 v[236:237], v8 offset:12288\n"
    "  ds_read_b64_tr_b16 v[238:239], v8 offset:14336\n"
    "  s_waitcnt lgkmcnt(4)\n"
    "  v_mfma_f32_32x32x16_bf16 v[96:111], v[224:227], v[216:219], 0\n"
    "  ds_read_b64_tr_b16 v[240:241], v8 offset:16384\n"
    "  ds_read_b64_tr_b16 v[242:243], v8 offset:18432\n"
    "  v_mfma_f32_32x32x16_bf16 v[96:111], v[228:231], v[220:223], v[96:111]\n"
    "  ds_read_b64_tr_b16 v[224:225], v8 offset:20480\n"
    "  ds_read_b64_tr_b16 v[226:227], v8 offset:22528\n"
    "  s_waitcnt lgkmcnt(4)\n"
    "  v_mfma_f32_32x32x16_bf16 v[96:111], v[232:235], v[128:131], v[96:111]\n"
    "  ds_read_b64_tr_b16 v[228:229], v8 offset:24576\n"
    "  ds_read_b64_tr_b16 v[230:231], v8 offset:26624\n"
    "  v_mfma_f32_32x32x16_bf16 v[96:111], v[236:239], v[132:135], v[96:111]\n"
    "  ds_read_b64_tr_b16 v[232:233], v8 offset:28672\n"
    "  ds_read_b64_tr_b16 v[234:235], v8 offset:30720\n"
    "  s_waitcnt lgkmcnt(4)\n"
    "  v_mfma_f32_32x32x16_bf16 v[96:111], v[240:243], v[136:139], v[96:111]\n"
    "  ds_read_b64_tr_b16 v[236:237], v8 offset:32768\n"
    "  ds_read_b64_tr_b16 v[238:239], v8 offset:34816\n"
    "  v_mfma_f32_32x32x16_bf16 v[96:111], v[224:227], v[140:143], v[96:111]\n"
    "  ds_read_b64_tr_b16 v[240:241], v8 offset:36864\n"
    "  ds_read_b64_tr_b16 v[242:243], v8 offset:38912\n"
    "  s_waitcnt lgkmcnt(4)\n"
    "  v_mfma_f32_32x32x16_bf16 v[96:111], v[228:231], v[144:147], v[96:111]\n"
    "  ds_read_b64_tr_b16 v[224:225], v5 offset:0\n"
    "  ds_read_b64_tr_b16 v[226:227], v5 offset:2048\n"
    "  v_mfma_f32_32x32x16_bf16 v[96:111], v[232:235], v[148:151], v[96:111]\n"
    "  ds_read_b64_tr_b16 v[228:229], v5 offset:4096\n"
    "  ds_read_b64_tr_b16 v[230:231], v5 offset:6144\n"
    "  s_waitcnt lgkmcnt(4)\n"
    "  v_mfma_f32_32x32x16_bf16 v[96:111], v[236:239], v[152:155], v[96:111]\n"
    "  ds_read_b64_tr_b16 v[232:233], v5 offset:8192\n"
    "  ds_read_b64_tr_b16 v[234:235], v5 offset:10240\n"
    "  v_mfma_f32_32x32x16_bf16 v[96:111], v[240:243], v[156:159], v[96:111]\n"
    "  ds_read_b64_tr_b16 v[236:237], v5 offset:12288\n"
    "  ds_read_b64_tr_b16 v[238:239], v5 offset:14336\n"
    "  s_waitcnt lgkmcnt(4)\n"
    "  v_mfma_f32_32x32x16_bf16 v[16:31], v[224:227], v[216:219], 0\n"
    "  ds_read_b64_tr_b16 v[240:241], v5 offset:16384\n"
    "  ds_read_b64_tr_b16 v[242:243], v5 offset:18432\n"
    "  v_mfma_f32_32x32x16_bf16 v[16:31], v[228:231], v[220:223], v[16:31]\n"
    "  ds_read_b64_tr_b16 v[224:225], v5 offset:20480\n"
    "  ds_read_b64_tr_b16 v[226:227], v5 offset:22528\n"
    "  s_waitcnt lgkmcnt(4)\n"
    "  v_mfma_f32_32x32x16_bf16 v[16:31], v[232:235], v[128:131], v[16:31]\n"
    "  ds_read_b64_tr_b16 v[228:229], v5 offset:24576\n"
    "  ds_read_b64_tr_b16 v[230:231], v5 offset:26624\n"
    "  s_waitcnt lgkmcnt(0)\n"
    "  s_load_dwordx8 s[68:75], s[24:25], 0x20 glc\n"
    "  s_mov_b32 s67, 0\n"
    ".Lgqa_fkd:\n"
    "  s_waitcnt lgkmcnt(0)\n"
    "  s_min_u32 s68, s68, s69\n"
    "  s_min_u32 s68, s68, s70\n"
    "  s_min_u32 s68, s68, s71\n"
    "  s_min_u32 s68, s68, s72\n"
    "  s_min_u32 s68, s68, s73\n"
    "  s_min_u32 s68, s68, s74\n"
    "  s_min_u32 s68, s68, s75\n"
    "  s_cmp_ge_u32 s68, 1\n"
    "  s_cbranch_scc1 .Lgqa_fkd_ok\n"
    "  s_add_u32 s67, s67, 1\n"
    "  s_cmp_gt_u32 s67, 0x8000\n"
    "  s_cbranch_scc1 .Lgqa_fkd_ok\n"
    "  s_load_dwordx8 s[68:75], s[24:25], 0x20 glc\n"
    "  s_branch .Lgqa_fkd\n"
    ".Lgqa_fkd_ok:\n"
    "  v_mov_b32 v116, v244\n"
    "  v_mov_b32 v117, v246\n"
    "  ds_write_b64 v114, v[116:117] offset:8192\n"
    "  ds_write_b128 v114, v[80:83] offset:0\n"
    "  ds_write_b128 v114, v[84:87] offset:1024\n"
    "  v_mfma_f32_32x32x16_bf16 v[16:31], v[236:239], v[132:135], v[16:31]\n"
    "  ds_read_b64_tr_b16 v[232:233], v5 offset:28672\n"
    "  ds_read_b64_tr_b16 v[234:235], v5 offset:30720\n"
    "  ds_write_b128 v114, v[88:91] offset:2048\n"
    "  ds_write_b128 v114, v[92:95] offset:3072\n"
    "  v_mfma_f32_32x32x16_bf16 v[16:31], v[240:243], v[136:139], v[16:31]\n"
    "  ds_read_b64_tr_b16 v[236:237], v5 offset:32768\n"
    "  ds_read_b64_tr_b16 v[238:239], v5 offset:34816\n"
    "  ds_write_b128 v114, v[96:99] offset:4096\n"
    "  ds_write_b128 v114, v[100:103] offset:5120\n"
    "  v_mfma_f32_32x32x16_bf16 v[16:31], v[224:227], v[140:143], v[16:31]\n"
    "  ds_read_b64_tr_b16 v[240:241], v5 offset:36864\n"
    "  ds_read_b64_tr_b16 v[242:243], v5 offset:38912\n"
    "  ds_write_b128 v114, v[104:107] offset:6144\n"
    "  ds_write_b128 v114, v[108:111] offset:7168\n"
    "  s_waitcnt lgkmcnt(10)\n"
    "  v_mfma_f32_32x32x16_bf16 v[16:31], v[228:231], v[144:147], v[16:31]\n"
    "  ds_read_b64_tr_b16 v[224:225], v6 offset:0\n"
    "  ds_read_b64_tr_b16 v[226:227], v6 offset:2048\n"
    "  v_mfma_f32_32x32x16_bf16 v[16:31], v[232:235], v[148:151], v[16:31]\n"
    "  ds_read_b64_tr_b16 v[228:229], v6 offset:4096\n"
    "  ds_read_b64_tr_b16 v[230:231], v6 offset:6144\n"
    "  s_waitcnt lgkmcnt(0)\n"
    "  v_mov_b32 v112, 1\n"
    "  v_lshlrev_b32 v113, 10, v13\n"
    "  buffer_store_dword v112, v113, s[20:23], s42 offen offset:16\n"
    "  v_mfma_f32_32x32x16_bf16 v[16:31], v[236:239], v[152:155], v[16:31]\n"
    "  ds_read_b64_tr_b16 v[232:233], v6 offset:8192\n"
    "  ds_read_b64_tr_b16 v[234:235], v6 offset:10240\n"
    "  v_mfma_f32_32x32x16_bf16 v[16:31], v[240:243], v[156:159], v[16:31]\n"
    "  ds_read_b64_tr_b16 v[236:237], v6 offset:12288\n"
    "  ds_read_b64_tr_b16 v[238:239], v6 offset:14336\n"
    "  v_mfma_f32_32x32x16_bf16 v[32:47], v[224:227], v[216:219], 0\n"
    "  ds_read_b64_tr_b16 v[240:241], v6 offset:16384\n"
    "  ds_read_b64_tr_b16 v[242:243], v6 offset:18432\n"
    "  v_mfma_f32_32x32x16_bf16 v[32:47], v[228:231], v[220:223], v[32:47]\n"
    "  ds_read_b64_tr_b16 v[224:225], v6 offset:20480\n"
    "  ds_read_b64_tr_b16 v[226:227], v6 offset:22528\n"
    "  s_waitcnt lgkmcnt(4)\n"
    "  v_mfma_f32_32x32x16_bf16 v[32:47], v[232:235], v[128:131], v[32:47]\n"
    "  ds_read_b64_tr_b16 v[228:229], v6 offset:24576\n"
    "  ds_read_b64_tr_b16 v[230:231], v6 offset:26624\n"
    "  v_mfma_f32_32x32x16_bf16 v[32:47], v[236:239], v[132:135], v[32:47]\n"
    "  ds_read_b64_tr_b16 v[232:233], v6 offset:28672\n"
    "  ds_read_b64_tr_b16 v[234:235], v6 offset:30720\n"
    "  s_waitcnt lgkmcnt(4)\n"
    "  v_mfma_f32_32x32x16_bf16 v[32:47], v[240:243], v[136:139], v[32:47]\n"
    "  ds_read_b64_tr_b16 v[236:237], v6 offset:32768\n"
    "  ds_read_b64_tr_b16 v[238:239], v6 offset:34816\n"
    "  v_mfma_f32_32x32x16_bf16 v[32:47], v[224:227], v[140:143], v[32:47]\n"
    "  ds_read_b64_tr_b16 v[240:241], v6 offset:36864\n"
    "  ds_read_b64_tr_b16 v[242:243], v6 offset:38912\n"
    "  s_waitcnt lgkmcnt(4)\n"
    "  v_mfma_f32_32x32x16_bf16 v[32:47], v[228:231], v[144:147], v[32:47]\n"
    "  v_mfma_f32_32x32x16_bf16 v[32:47], v[232:235], v[148:151], v[32:47]\n"
    "  s_waitcnt lgkmcnt(0)\n"
    "  v_mfma_f32_32x32x16_bf16 v[32:47], v[236:239], v[152:155], v[32:47]\n"
    "  s_load_dword s68, s[24:25], s45 glc\n"
    "  v_mfma_f32_32x32x16_bf16 v[32:47], v[240:243], v[156:159], v[32:47]\n"
    "  s_mov_b32 s67, 0\n"
    ".Lgqa_fx:\n"
    "  s_waitcnt lgkmcnt(0)\n"
    "  s_cmp_ge_u32 s68, 1\n"
    "  s_cbranch_scc1 .Lgqa_fx_ok\n"
    "  s_add_u32 s67, s67, 1\n"
    "  s_cmp_gt_u32 s67, 0x8000\n"
    "  s_cbranch_scc1 .Lgqa_fx_ok\n"
    "  s_load_dword s68, s[24:25], s45 glc\n"
    "  s_branch .Lgqa_fx\n"
    ".Lgqa_fx_ok:\n"
    "  ds_read_b64 v[252:253], v9 offset:8192\n"
    "  ds_read_b128 v[128:131], v9 offset:0\n"
    "  ds_read_b128 v[132:135], v9 offset:1024\n"
    "  ds_read_b128 v[136:139], v9 offset:2048\n"
    "  ds_read_b128 v[140:143], v9 offset:3072\n"
    "  ds_read_b128 v[144:147], v9 offset:4096\n"
    "  ds_read_b128 v[148:151], v9 offset:5120\n"
    "  ds_read_b128 v[152:155], v9 offset:6144\n"
    "  ds_read_b128 v[156:159], v9 offset:7168\n"
    "  s_waitcnt lgkmcnt(8)\n"
    "  v_max_f32 v254, v244, v252\n"
    "  v_sub_f32 v250, v244, v254\n"
    "  v_sub_f32 v251, v252, v254\n"
    "  v_mul_f32 v250, 0x3e0293ee, v250\n"
    "  v_mul_f32 v251, 0x3e0293ee, v251\n"
    "  v_exp_f32 v250, v250\n"
    "  v_exp_f32 v251, v251\n"
    "  v_mul_f32 v246, v246, v250\n"
    "  v_fmac_f32 v246, v253, v251\n"
    "  v_rcp_f32 v246, v246\n"
    "  s_nop 0\n"
    "  v_mul_f32 v250, v250, v246\n"
    "  v_mul_f32 v251, v251, v246\n"
    "  v_add_u32 v9, s29, v10\n"
    "  v_mul_f32 v16, v16, v250\n"
    "  v_mul_f32 v17, v17, v250\n"
    "  v_mul_f32 v18, v18, v250\n"
    "  v_mul_f32 v19, v19, v250\n"
    "  v_mul_f32 v20, v20, v250\n"
    "  v_mul_f32 v21, v21, v250\n"
    "  v_mul_f32 v22, v22, v250\n"
    "  v_mul_f32 v23, v23, v250\n"
    "  v_mul_f32 v24, v24, v250\n"
    "  v_mul_f32 v25, v25, v250\n"
    "  v_mul_f32 v26, v26, v250\n"
    "  v_mul_f32 v27, v27, v250\n"
    "  v_mul_f32 v28, v28, v250\n"
    "  v_mul_f32 v29, v29, v250\n"
    "  v_mul_f32 v30, v30, v250\n"
    "  v_mul_f32 v31, v31, v250\n"
    "  v_mul_f32 v32, v32, v250\n"
    "  v_mul_f32 v33, v33, v250\n"
    "  v_mul_f32 v34, v34, v250\n"
    "  v_mul_f32 v35, v35, v250\n"
    "  v_mul_f32 v36, v36, v250\n"
    "  v_mul_f32 v37, v37, v250\n"
    "  v_mul_f32 v38, v38, v250\n"
    "  v_mul_f32 v39, v39, v250\n"
    "  v_mul_f32 v40, v40, v250\n"
    "  v_mul_f32 v41, v41, v250\n"
    "  v_mul_f32 v42, v42, v250\n"
    "  v_mul_f32 v43, v43, v250\n"
    "  v_mul_f32 v44, v44, v250\n"
    "  v_mul_f32 v45, v45, v250\n"
    "  v_mul_f32 v46, v46, v250\n"
    "  v_mul_f32 v47, v47, v250\n"
    "  s_waitcnt lgkmcnt(7)\n"
    "  v_fmac_f32 v16, v128, v251\n"
    "  v_fmac_f32 v17, v129, v251\n"
    "  v_fmac_f32 v18, v130, v251\n"
    "  v_fmac_f32 v19, v131, v251\n"
    "  s_waitcnt lgkmcnt(6)\n"
    "  v_fmac_f32 v20, v132, v251\n"
    "  v_fmac_f32 v21, v133, v251\n"
    "  v_fmac_f32 v22, v134, v251\n"
    "  v_fmac_f32 v23, v135, v251\n"
    "  s_waitcnt lgkmcnt(5)\n"
    "  v_fmac_f32 v24, v136, v251\n"
    "  v_fmac_f32 v25, v137, v251\n"
    "  v_fmac_f32 v26, v138, v251\n"
    "  v_fmac_f32 v27, v139, v251\n"
    "  s_waitcnt lgkmcnt(4)\n"
    "  v_fmac_f32 v28, v140, v251\n"
    "  v_fmac_f32 v29, v141, v251\n"
    "  v_fmac_f32 v30, v142, v251\n"
    "  v_fmac_f32 v31, v143, v251\n"
    "  s_waitcnt lgkmcnt(3)\n"
    "  v_fmac_f32 v32, v144, v251\n"
    "  v_fmac_f32 v33, v145, v251\n"
    "  v_fmac_f32 v34, v146, v251\n"
    "  v_fmac_f32 v35, v147, v251\n"
    "  s_waitcnt lgkmcnt(2)\n"
    "  v_fmac_f32 v36, v148, v251\n"
    "  v_fmac_f32 v37, v149, v251\n"
    "  v_fmac_f32 v38, v150, v251\n"
    "  v_fmac_f32 v39, v151, v251\n"
    "  s_waitcnt lgkmcnt(1)\n"
    "  v_fmac_f32 v40, v152, v251\n"
    "  v_fmac_f32 v41, v153, v251\n"
    "  v_fmac_f32 v42, v154, v251\n"
    "  v_fmac_f32 v43, v155, v251\n"
    "  s_waitcnt lgkmcnt(0)\n"
    "  v_fmac_f32 v44, v156, v251\n"
    "  v_fmac_f32 v45, v157, v251\n"
    "  v_fmac_f32 v46, v158, v251\n"
    "  v_fmac_f32 v47, v159, v251\n"
    "  v_cvt_pk_bf16_f32 v112, v16, v17\n"
    "  v_cvt_pk_bf16_f32 v116, v24, v25\n"
    "  v_cvt_pk_bf16_f32 v120, v32, v33\n"
    "  v_cvt_pk_bf16_f32 v124, v40, v41\n"
    "  v_cvt_pk_bf16_f32 v113, v18, v19\n"
    "  v_cvt_pk_bf16_f32 v117, v26, v27\n"
    "  v_cvt_pk_bf16_f32 v121, v34, v35\n"
    "  v_cvt_pk_bf16_f32 v125, v42, v43\n"
    "  s_mov_b64 vcc, s[58:59]\n"
    "  v_cvt_pk_bf16_f32 v114, v20, v21\n"
    "  v_cvt_pk_bf16_f32 v118, v28, v29\n"
    "  v_cvt_pk_bf16_f32 v122, v36, v37\n"
    "  v_cndmask_b32_dpp v208, v116, v112, vcc quad_perm:[1,0,3,2] row_mask:0xf bank_mask:0xf\n"
    "  v_cvt_pk_bf16_f32 v126, v44, v45\n"
    "  v_cvt_pk_bf16_f32 v115, v22, v23\n"
    "  v_cvt_pk_bf16_f32 v119, v30, v31\n"
    "  v_cndmask_b32_dpp v209, v117, v113, vcc quad_perm:[1,0,3,2] row_mask:0xf bank_mask:0xf\n"
    "  v_cvt_pk_bf16_f32 v123, v38, v39\n"
    "  v_cvt_pk_bf16_f32 v127, v46, v47\n"
    "  v_cndmask_b32_dpp v216, v124, v120, vcc quad_perm:[1,0,3,2] row_mask:0xf bank_mask:0xf\n"
    "  v_mov_b32 v255, v255\n"
    "  v_cndmask_b32_dpp v217, v125, v121, vcc quad_perm:[1,0,3,2] row_mask:0xf bank_mask:0xf\n"
    "  s_mov_b64 vcc, s[60:61]\n"
    "  v_cndmask_b32_dpp v212, v112, v116, vcc quad_perm:[1,0,3,2] row_mask:0xf bank_mask:0xf\n"
    "  v_mov_b32 v255, v255\n"
    "  v_cndmask_b32_dpp v213, v113, v117, vcc quad_perm:[1,0,3,2] row_mask:0xf bank_mask:0xf\n"
    "  v_mov_b32 v255, v255\n"
    "  v_cndmask_b32_dpp v220, v120, v124, vcc quad_perm:[1,0,3,2] row_mask:0xf bank_mask:0xf\n"
    "  v_mov_b32 v255, v255\n"
    "  v_cndmask_b32_dpp v221, v121, v125, vcc quad_perm:[1,0,3,2] row_mask:0xf bank_mask:0xf\n"
    "  v_mov_b32 v255, v255\n"
    "  v_cndmask_b32_dpp v214, v114, v118, vcc quad_perm:[1,0,3,2] row_mask:0xf bank_mask:0xf\n"
    "  v_mov_b32 v255, v255\n"
    "  v_cndmask_b32_dpp v215, v115, v119, vcc quad_perm:[1,0,3,2] row_mask:0xf bank_mask:0xf\n"
    "  v_mov_b32 v255, v255\n"
    "  v_cndmask_b32_dpp v222, v122, v126, vcc quad_perm:[1,0,3,2] row_mask:0xf bank_mask:0xf\n"
    "  v_mov_b32 v255, v255\n"
    "  v_cndmask_b32_dpp v223, v123, v127, vcc quad_perm:[1,0,3,2] row_mask:0xf bank_mask:0xf\n"
    "  s_mov_b64 vcc, s[58:59]\n"
    "  v_cndmask_b32_dpp v210, v118, v114, vcc quad_perm:[1,0,3,2] row_mask:0xf bank_mask:0xf\n"
    "  v_mov_b32 v255, v255\n"
    "  v_cndmask_b32_dpp v211, v119, v115, vcc quad_perm:[1,0,3,2] row_mask:0xf bank_mask:0xf\n"
    "  v_mov_b32 v255, v255\n"
    "  v_cndmask_b32_dpp v218, v126, v122, vcc quad_perm:[1,0,3,2] row_mask:0xf bank_mask:0xf\n"
    "  v_mov_b32 v255, v255\n"
    "  v_cndmask_b32_dpp v219, v127, v123, vcc quad_perm:[1,0,3,2] row_mask:0xf bank_mask:0xf\n"
    "  s_mov_b64 vcc, s[62:63]\n"
    "  v_cndmask_b32_dpp v112, v216, v208, vcc quad_perm:[2,3,0,1] row_mask:0xf bank_mask:0xf\n"
    "  v_mov_b32 v255, v255\n"
    "  v_cndmask_b32_dpp v113, v217, v209, vcc quad_perm:[2,3,0,1] row_mask:0xf bank_mask:0xf\n"
    "  v_mov_b32 v255, v255\n"
    "  v_cndmask_b32_dpp v114, v218, v210, vcc quad_perm:[2,3,0,1] row_mask:0xf bank_mask:0xf\n"
    "  v_mov_b32 v255, v255\n"
    "  v_cndmask_b32_dpp v115, v219, v211, vcc quad_perm:[2,3,0,1] row_mask:0xf bank_mask:0xf\n"
    "  v_mov_b32 v255, v255\n"
    "  v_cndmask_b32_dpp v116, v220, v212, vcc quad_perm:[2,3,0,1] row_mask:0xf bank_mask:0xf\n"
    "  v_mov_b32 v255, v255\n"
    "  v_cndmask_b32_dpp v117, v221, v213, vcc quad_perm:[2,3,0,1] row_mask:0xf bank_mask:0xf\n"
    "  v_mov_b32 v255, v255\n"
    "  v_cndmask_b32_dpp v118, v222, v214, vcc quad_perm:[2,3,0,1] row_mask:0xf bank_mask:0xf\n"
    "  v_mov_b32 v255, v255\n"
    "  v_cndmask_b32_dpp v119, v223, v215, vcc quad_perm:[2,3,0,1] row_mask:0xf bank_mask:0xf\n"
    "  s_mov_b64 vcc, s[64:65]\n"
    "  v_cndmask_b32_dpp v120, v208, v216, vcc quad_perm:[2,3,0,1] row_mask:0xf bank_mask:0xf\n"
    "  v_mov_b32 v255, v255\n"
    "  v_cndmask_b32_dpp v121, v209, v217, vcc quad_perm:[2,3,0,1] row_mask:0xf bank_mask:0xf\n"
    "  v_mov_b32 v255, v255\n"
    "  v_cndmask_b32_dpp v122, v210, v218, vcc quad_perm:[2,3,0,1] row_mask:0xf bank_mask:0xf\n"
    "  v_mov_b32 v255, v255\n"
    "  v_cndmask_b32_dpp v123, v211, v219, vcc quad_perm:[2,3,0,1] row_mask:0xf bank_mask:0xf\n"
    "  v_mov_b32 v255, v255\n"
    "  v_cndmask_b32_dpp v124, v212, v220, vcc quad_perm:[2,3,0,1] row_mask:0xf bank_mask:0xf\n"
    "  v_mov_b32 v255, v255\n"
    "  v_cndmask_b32_dpp v125, v213, v221, vcc quad_perm:[2,3,0,1] row_mask:0xf bank_mask:0xf\n"
    "  v_mov_b32 v255, v255\n"
    "  v_cndmask_b32_dpp v126, v214, v222, vcc quad_perm:[2,3,0,1] row_mask:0xf bank_mask:0xf\n"
    "  v_mov_b32 v255, v255\n"
    "  v_cndmask_b32_dpp v127, v215, v223, vcc quad_perm:[2,3,0,1] row_mask:0xf bank_mask:0xf\n"
    "  buffer_store_dwordx4 v[112:115], v9, s[16:19], 0 offen offset:0 sc1\n"
    "  buffer_store_dwordx4 v[116:119], v9, s[16:19], 0 offen offset:256 sc1\n"
    "  buffer_store_dwordx4 v[120:123], v9, s[16:19], 0 offen offset:512 sc1\n"
    "  buffer_store_dwordx4 v[124:127], v9, s[16:19], 0 offen offset:768 sc1\n"
    "  s_branch .Lgqa_end\n"
    ".Lgqa_bepi:\n"
    "  v_mul_f32 v80, v80, v249\n"
    "  v_mul_f32 v81, v81, v249\n"
    "  v_cvt_pk_bf16_f32 v16, v80, v81\n"
    "  v_mul_f32 v88, v88, v249\n"
    "  v_mul_f32 v89, v89, v249\n"
    "  v_cvt_pk_bf16_f32 v20, v88, v89\n"
    "  v_mul_f32 v96, v96, v249\n"
    "  v_mul_f32 v97, v97, v249\n"
    "  v_cvt_pk_bf16_f32 v24, v96, v97\n"
    "  v_mul_f32 v104, v104, v249\n"
    "  v_mul_f32 v105, v105, v249\n"
    "  v_cvt_pk_bf16_f32 v28, v104, v105\n"
    "  v_mul_f32 v82, v82, v249\n"
    "  v_mul_f32 v83, v83, v249\n"
    "  v_cvt_pk_bf16_f32 v17, v82, v83\n"
    "  v_mul_f32 v90, v90, v249\n"
    "  v_mul_f32 v91, v91, v249\n"
    "  v_cvt_pk_bf16_f32 v21, v90, v91\n"
    "  v_mul_f32 v98, v98, v249\n"
    "  v_mul_f32 v99, v99, v249\n"
    "  v_cvt_pk_bf16_f32 v25, v98, v99\n"
    "  v_mul_f32 v106, v106, v249\n"
    "  v_mul_f32 v107, v107, v249\n"
    "  v_cvt_pk_bf16_f32 v29, v106, v107\n"
    "  s_mov_b64 vcc, s[58:59]\n"
    "  v_mul_f32 v84, v84, v249\n"
    "  v_mul_f32 v85, v85, v249\n"
    "  v_cvt_pk_bf16_f32 v18, v84, v85\n"
    "  v_cndmask_b32_dpp v32, v20, v16, vcc quad_perm:[1,0,3,2] row_mask:0xf bank_mask:0xf\n"
    "  v_mul_f32 v92, v92, v249\n"
    "  v_mul_f32 v93, v93, v249\n"
    "  v_cvt_pk_bf16_f32 v22, v92, v93\n"
    "  v_cndmask_b32_dpp v33, v21, v17, vcc quad_perm:[1,0,3,2] row_mask:0xf bank_mask:0xf\n"
    "  v_mul_f32 v100, v100, v249\n"
    "  v_mul_f32 v101, v101, v249\n"
    "  v_cvt_pk_bf16_f32 v26, v100, v101\n"
    "  v_cndmask_b32_dpp v40, v28, v24, vcc quad_perm:[1,0,3,2] row_mask:0xf bank_mask:0xf\n"
    "  v_mul_f32 v108, v108, v249\n"
    "  v_mul_f32 v109, v109, v249\n"
    "  v_cvt_pk_bf16_f32 v30, v108, v109\n"
    "  v_cndmask_b32_dpp v41, v29, v25, vcc quad_perm:[1,0,3,2] row_mask:0xf bank_mask:0xf\n"
    "  v_mul_f32 v86, v86, v249\n"
    "  v_mul_f32 v87, v87, v249\n"
    "  v_cvt_pk_bf16_f32 v19, v86, v87\n"
    "  s_mov_b64 vcc, s[60:61]\n"
    "  v_mul_f32 v94, v94, v249\n"
    "  v_mul_f32 v95, v95, v249\n"
    "  v_cvt_pk_bf16_f32 v23, v94, v95\n"
    "  v_cndmask_b32_dpp v36, v16, v20, vcc quad_perm:[1,0,3,2] row_mask:0xf bank_mask:0xf\n"
    "  v_mul_f32 v102, v102, v249\n"
    "  v_mul_f32 v103, v103, v249\n"
    "  v_cvt_pk_bf16_f32 v27, v102, v103\n"
    "  v_cndmask_b32_dpp v37, v17, v21, vcc quad_perm:[1,0,3,2] row_mask:0xf bank_mask:0xf\n"
    "  v_mul_f32 v110, v110, v249\n"
    "  v_mul_f32 v111, v111, v249\n"
    "  v_cvt_pk_bf16_f32 v31, v110, v111\n"
    "  v_cndmask_b32_dpp v44, v24, v28, vcc quad_perm:[1,0,3,2] row_mask:0xf bank_mask:0xf\n"
    "  v_mov_b32 v255, v255\n"
    "  v_cndmask_b32_dpp v45, v25, v29, vcc quad_perm:[1,0,3,2] row_mask:0xf bank_mask:0xf\n"
    "  v_mov_b32 v255, v255\n"
    "  v_cndmask_b32_dpp v38, v18, v22, vcc quad_perm:[1,0,3,2] row_mask:0xf bank_mask:0xf\n"
    "  v_mov_b32 v255, v255\n"
    "  v_cndmask_b32_dpp v39, v19, v23, vcc quad_perm:[1,0,3,2] row_mask:0xf bank_mask:0xf\n"
    "  v_mov_b32 v255, v255\n"
    "  v_cndmask_b32_dpp v46, v26, v30, vcc quad_perm:[1,0,3,2] row_mask:0xf bank_mask:0xf\n"
    "  v_mov_b32 v255, v255\n"
    "  v_cndmask_b32_dpp v47, v27, v31, vcc quad_perm:[1,0,3,2] row_mask:0xf bank_mask:0xf\n"
    "  s_mov_b64 vcc, s[58:59]\n"
    "  v_cndmask_b32_dpp v34, v22, v18, vcc quad_perm:[1,0,3,2] row_mask:0xf bank_mask:0xf\n"
    "  v_mov_b32 v255, v255\n"
    "  v_cndmask_b32_dpp v35, v23, v19, vcc quad_perm:[1,0,3,2] row_mask:0xf bank_mask:0xf\n"
    "  v_mov_b32 v255, v255\n"
    "  v_cndmask_b32_dpp v42, v30, v26, vcc quad_perm:[1,0,3,2] row_mask:0xf bank_mask:0xf\n"
    "  v_mov_b32 v255, v255\n"
    "  v_cndmask_b32_dpp v43, v31, v27, vcc quad_perm:[1,0,3,2] row_mask:0xf bank_mask:0xf\n"
    "  s_mov_b64 vcc, s[62:63]\n"
    "  v_cndmask_b32_dpp v16, v40, v32, vcc quad_perm:[2,3,0,1] row_mask:0xf bank_mask:0xf\n"
    "  v_mov_b32 v255, v255\n"
    "  v_cndmask_b32_dpp v17, v41, v33, vcc quad_perm:[2,3,0,1] row_mask:0xf bank_mask:0xf\n"
    "  v_mov_b32 v255, v255\n"
    "  v_cndmask_b32_dpp v18, v42, v34, vcc quad_perm:[2,3,0,1] row_mask:0xf bank_mask:0xf\n"
    "  v_mov_b32 v255, v255\n"
    "  v_cndmask_b32_dpp v19, v43, v35, vcc quad_perm:[2,3,0,1] row_mask:0xf bank_mask:0xf\n"
    "  v_mov_b32 v255, v255\n"
    "  v_cndmask_b32_dpp v20, v44, v36, vcc quad_perm:[2,3,0,1] row_mask:0xf bank_mask:0xf\n"
    "  v_mov_b32 v255, v255\n"
    "  v_cndmask_b32_dpp v21, v45, v37, vcc quad_perm:[2,3,0,1] row_mask:0xf bank_mask:0xf\n"
    "  v_mov_b32 v255, v255\n"
    "  v_cndmask_b32_dpp v22, v46, v38, vcc quad_perm:[2,3,0,1] row_mask:0xf bank_mask:0xf\n"
    "  v_mov_b32 v255, v255\n"
    "  v_cndmask_b32_dpp v23, v47, v39, vcc quad_perm:[2,3,0,1] row_mask:0xf bank_mask:0xf\n"
    "  s_mov_b64 vcc, s[64:65]\n"
    "  v_cndmask_b32_dpp v24, v32, v40, vcc quad_perm:[2,3,0,1] row_mask:0xf bank_mask:0xf\n"
    "  v_mov_b32 v255, v255\n"
    "  v_cndmask_b32_dpp v25, v33, v41, vcc quad_perm:[2,3,0,1] row_mask:0xf bank_mask:0xf\n"
    "  v_mov_b32 v255, v255\n"
    "  v_cndmask_b32_dpp v26, v34, v42, vcc quad_perm:[2,3,0,1] row_mask:0xf bank_mask:0xf\n"
    "  v_mov_b32 v255, v255\n"
    "  v_cndmask_b32_dpp v27, v35, v43, vcc quad_perm:[2,3,0,1] row_mask:0xf bank_mask:0xf\n"
    "  v_mov_b32 v255, v255\n"
    "  v_cndmask_b32_dpp v28, v36, v44, vcc quad_perm:[2,3,0,1] row_mask:0xf bank_mask:0xf\n"
    "  v_mov_b32 v255, v255\n"
    "  v_cndmask_b32_dpp v29, v37, v45, vcc quad_perm:[2,3,0,1] row_mask:0xf bank_mask:0xf\n"
    "  v_mov_b32 v255, v255\n"
    "  v_cndmask_b32_dpp v30, v38, v46, vcc quad_perm:[2,3,0,1] row_mask:0xf bank_mask:0xf\n"
    "  v_mov_b32 v255, v255\n"
    "  v_cndmask_b32_dpp v31, v39, v47, vcc quad_perm:[2,3,0,1] row_mask:0xf bank_mask:0xf\n"
    "  buffer_store_dwordx4 v[16:19], v9, s[16:19], 0 offen offset:128 sc1\n"
    "  buffer_store_dwordx4 v[20:23], v9, s[16:19], 0 offen offset:384 sc1\n"
    "  buffer_store_dwordx4 v[24:27], v9, s[16:19], 0 offen offset:640 sc1\n"
    "  buffer_store_dwordx4 v[28:31], v9, s[16:19], 0 offen offset:896 sc1\n"
    ".Lgqa_end:\n"
    :
    : [ka] "s"(__builtin_amdgcn_kernarg_segment_ptr()), [bid] "s"(blockIdx.x), [ldsb] "s"(ldsb), [tid] "v"(threadIdx.x)
    : "memory", "scc", "vcc",
      "v1", "v2", "v3", "v4", "v5", "v6", "v7", "v8", "v9", "v10", "v11", "v12", "v13", "v14", "v15", "v16", "v17", "v18", "v19", "v20", "v21", "v22", "v23", "v24",
      "v25", "v26", "v27", "v28", "v29", "v30", "v31", "v32", "v33", "v34", "v35", "v36", "v37", "v38", "v39", "v40", "v41", "v42", "v43", "v44", "v45", "v46", "v47", "v48",
      "v49", "v50", "v51", "v52", "v53", "v54", "v55", "v56", "v57", "v58", "v59", "v60", "v61", "v62", "v63", "v64", "v65", "v66", "v67", "v68", "v69", "v70", "v71", "v72",
      "v73", "v74", "v75", "v76", "v77", "v78", "v79", "v80", "v81", "v82", "v83", "v84", "v85", "v86", "v87", "v88", "v89", "v90", "v91", "v92", "v93", "v94", "v95", "v96",
      "v97", "v98", "v99", "v100", "v101", "v102", "v103", "v104", "v105", "v106", "v107", "v108", "v109", "v110", "v111", "v112", "v113", "v114", "v115", "v116", "v117", "v118", "v119", "v120",
      "v121", "v122", "v123", "v124", "v125", "v126", "v127", "v128", "v129", "v130", "v131", "v132", "v133", "v134", "v135", "v136", "v137", "v138", "v139", "v140", "v141", "v142", "v143", "v144",
      "v145", "v146", "v147", "v148", "v149", "v150", "v151", "v152", "v153", "v154", "v155", "v156", "v157", "v158", "v159", "v160", "v161", "v162", "v163", "v164", "v165", "v166", "v167", "v168",
      "v169", "v170", "v171", "v172", "v173", "v174", "v175", "v176", "v177", "v178", "v179", "v180", "v181", "v182", "v183", "v184", "v185", "v186", "v187", "v188", "v189", "v190", "v191", "v192",
      "v193", "v194", "v195", "v196", "v197", "v198", "v199", "v200", "v201", "v202", "v203", "v204", "v205", "v206", "v207", "v208", "v209", "v210", "v211", "v212", "v213", "v214", "v215", "v216",
      "v217", "v218", "v219", "v220", "v221", "v222", "v223", "v224", "v225", "v226", "v227", "v228", "v229", "v230", "v231", "v232", "v233", "v234", "v235", "v236", "v237", "v238", "v239", "v240",
      "v241", "v242", "v243", "v244", "v245", "v246", "v247", "v248", "v249", "v250", "v251", "v252", "v253", "v254", "v255", "s4", "s5", "s6", "s7", "s8", "s9", "s10", "s11", "s12",
      "s13", "s14", "s15", "s16", "s17", "s18", "s19", "s20", "s21", "s22", "s23", "s24", "s25", "s26", "s27", "s28", "s29", "s30", "s31", "s36", "s37", "s38", "s39", "s40",
      "s41", "s42", "s43", "s44", "s45", "s46", "s47", "s48", "s49", "s50", "s51", "s52", "s53", "s54", "s55", "s56", "s57", "s58", "s59", "s60", "s61", "s62", "s63", "s64",
      "s65", "s66", "s67", "s68", "s69", "s70", "s71", "s72", "s73", "s74", "s75", "s76", "s77");
}

extern "C" void kernel_launch(void* const* d_in, const int* in_sizes, int n_in,
                              void* d_out, int out_size, void* d_ws, size_t ws_size,
                              hipStream_t stream) {
  gqa_attn_kernel<<<dim3(256), dim3(512), 0, stream>>>((const unsigned short*)d_in[0], (const unsigned short*)d_in[1],
                                                      (const unsigned short*)d_in[2], (unsigned short*)d_out, (unsigned*)d_ws);
}
```
